```python
import torch

def _softmax(x, axis=-1):
    return torch.softmax(x, dim=axis)
def _gelu(x):
    return torch.nn.functional.gelu(x, approximate="tanh")
def _silu(x):
    return torch.nn.functional.silu(x)
def _sigmoid(x):
    return torch.sigmoid(x)
def _layernorm(x, axis=-1, eps=1e-5):
    m = x.mean(dim=axis, keepdim=True)
    v = x.var(dim=axis, keepdim=True, unbiased=False)
    return (x - m) / torch.sqrt(v + eps)
def _scatter_add(tgt, idx, src, axis=0):
    return tgt.clone().index_add_(axis, idx, src)
def _segment_sum(src, seg, n):
    out = torch.zeros((n,) + src.shape[1:], dtype=src.dtype)
    return out.index_add_(0, seg, src)
def _pad(x, pw):
    flat = tuple(v for pair in reversed(pw) for v in pair)
    return torch.nn.functional.pad(x, flat)

kernel_name = 'sequential_preLN__sliding_block_cached__dense_activation_silu__decode'

def setup_inputs():

    _w = (lambda a: (lambda t: t.to(torch.bfloat16) if t.is_floating_point() else t)(torch.as_tensor(a)))
    def _A_setup():
        B, Sq, D = 2, 1, 512
        Hq, Hkv, Sk, Dh = 32, 8, 8192, 128
        assert Hq % Hkv == 0, (Hq, Hkv)
        return {
            "x": _w((lambda *s: torch.randn(*s, dtype=torch.bfloat16) * 0.05)(B, Sq, D)),
            "k_cache": _w((lambda *s: torch.randn(*s, dtype=torch.bfloat16) * 3.2)(B, Hkv, Sk, Dh)),
            "v_cache": _w((lambda *s: torch.randn(*s, dtype=torch.bfloat16) * 0.8)(B, Hkv, Sk, Dh)),
            "wq": _w((lambda *s: torch.randn(*s, dtype=torch.bfloat16) * 3.2)(D, Hq * Dh) * (D ** -0.5)),
            "wo": _w((lambda *s: torch.randn(*s, dtype=torch.bfloat16) * 0.8)(Hq * Dh, D) * ((Hq * Dh) ** -0.5)),
        }
    _A_in = _A_setup()

    def _B_setup():
        B, S, D, F = 2, 1, 512, 1792
        return {
            "x": _w((lambda *s: torch.randn(*s, dtype=torch.bfloat16) * 0.05)(B, S, D)),
            "w1": _w((lambda *s: torch.randn(*s, dtype=torch.bfloat16) * 3.2)(D, F) * (D ** -0.5)),
            "w2": _w((lambda *s: torch.randn(*s, dtype=torch.bfloat16) * 0.4)(F, D) * (F ** -0.5)),
        }
    _B_in = _B_setup()

    return {
        "A_x": _A_in["x"],
        "A_k_cache": _A_in["k_cache"],
        "A_v_cache": _A_in["v_cache"],
        "A_wq": _A_in["wq"],
        "A_wo": _A_in["wo"],
        "B_x": _B_in["x"],
        "B_w1": _B_in["w1"],
        "B_w2": _B_in["w2"],
    }

def reference(A_x, A_k_cache, A_v_cache, A_wq, A_wo, B_x, B_w1, B_w2):
    def _A_ref(x, k_cache, v_cache, wq, wo):
        B, Sq, D = x.shape
        Hq, Dh = 32, 128
        Hkv, Sk = k_cache.shape[1], k_cache.shape[2]
        W = min(4096, max(256, Sk // 4))
        rep = Hq // Hkv
        q = (lambda x, *d: x.permute(*d))((x @ wq).reshape(B, Sq, Hq, Dh), 0, 2, 1, 3)
        k = (lambda x, n, axis: torch.repeat_interleave(x, n, dim=axis))(k_cache[:, :, Sk - W:, :], rep, axis=1)
        v = (lambda x, n, axis: torch.repeat_interleave(x, n, dim=axis))(v_cache[:, :, Sk - W:, :], rep, axis=1)
        scores = torch.einsum("bhqd,bhkd->bhqk", q, k) * (Dh ** -0.5)
        p = _softmax(scores, axis=-1)
        o = torch.einsum("bhqk,bhkd->bhqd", p, v)
        return (lambda x, *d: x.permute(*d))(o, 0, 2, 1, 3).reshape(B, Sq, Hq * Dh) @ wo

    def _B_ref(x, w1, w2):
        h = torch.einsum("bsd,df->bsf", x, w1)
        h = _silu(h)
        out = torch.einsum("bsf,fd->bsd", h, w2)
        return out

    x = A_x
    h = x + _A_ref(_layernorm(x, axis=-1), A_k_cache, A_v_cache, A_wq, A_wo)
    h2 = h + _B_ref(_layernorm(h, axis=-1), B_w1, B_w2)
    return h2
```

```cpp
#include <hip/hip_runtime.h>
#include <hip/hip_bf16.h>
#define MIMIC 1
typedef unsigned short u16;
__device__ __forceinline__ float bf2f(u16 b) { return __uint_as_float(((unsigned)b) << 16); }
__device__ __forceinline__ u16 f2bf(float f) { unsigned u = __float_as_uint(f); u += 0x7fffu + ((u >> 16) & 1u); return (u16)(u >> 16); }
__device__ __forceinline__ float rb(float f) {
  return bf2f(f2bf(f));
}
constexpr int D = 512, HQ = 32, HKV = 8, SK = 8192, DH = 128, W = 2048, F = 1792, NS = 16, KPS = W / NS;

__device__ void block_ln(const float* hrow  , float* out  , float* red) {
  int t = threadIdx.x;
  float a = hrow[t], b = hrow[t + 256];
  float s = a + b;
  for (int o = 32; o >= 1; o >>= 1) s += __shfl_xor(s, o);
  if ((t & 63) == 0) red[t >> 6] = s;
  __syncthreads();
  float m = (red[0] + red[1] + red[2] + red[3]) * (1.f / 512.f);
  __syncthreads();
  float da = a - m, db = b - m;
  float v = da * da + db * db;
  for (int o = 32; o >= 1; o >>= 1) v += __shfl_xor(v, o);
  if ((t & 63) == 0) red[t >> 6] = v;
  __syncthreads();
  v = (red[0] + red[1] + red[2] + red[3]) * (1.f / 512.f);
  __syncthreads();
  float mr = rb(m), vr = rb(v);
  float den = rb(sqrtf(rb(vr + 1e-5f)));
  out[t] = rb(rb(a - mr) / den);
  out[t + 256] = rb(rb(b - mr) / den);
  __syncthreads();
}

__global__ void k_q(const u16* x, const u16* wq, float* q) {
  __shared__ float xr[2][512], xn[2][512], red[4];
  int t = threadIdx.x;
  for (int b = 0; b < 2; ++b) { xr[b][t] = bf2f(x[b * 512 + t]); xr[b][t + 256] = bf2f(x[b * 512 + t + 256]); }
  __syncthreads();
  block_ln(xr[0], xn[0], red); block_ln(xr[1], xn[1], red);
  int n = blockIdx.x * 256 + t;
  float a0 = 0, a1 = 0;
  for (int d = 0; d < 512; ++d) { float w = bf2f(wq[(size_t)d * 4096 + n]); a0 += xn[0][d] * w; a1 += xn[1][d] * w; }
  q[n] = rb(a0); q[4096 + n] = rb(a1);
}
__global__ void k_attn(const float* q, const u16* kc, const u16* vc, float* o) {
  __shared__ float qs[128], sc[W], red[4];
  int t = threadIdx.x, bh = blockIdx.x, b = bh / HQ, h = bh % HQ, g = h / 4;
  if (t < 128) qs[t] = q[b * 4096 + h * 128 + t];
  __syncthreads();
  const u16* kb = kc + ((size_t)(b * HKV + g) * SK + (SK - W)) * DH;
  const u16* vb = vc + ((size_t)(b * HKV + g) * SK + (SK - W)) * DH;
  float mx = -INFINITY;
  for (int k = t; k < W; k += 256) {
    float s = 0;
    for (int d = 0; d < 128; ++d) s += qs[d] * bf2f(kb[(size_t)k * DH + d]);
    s = rb(rb(s) * rb(0.08838834764831845f));
    sc[k] = s; mx = fmaxf(mx, s);
  }
  for (int of = 32; of >= 1; of >>= 1) mx = fmaxf(mx, __shfl_xor(mx, of));
  if ((t & 63) == 0) red[t >> 6] = mx;
  __syncthreads();
  mx = fmaxf(fmaxf(red[0], red[1]), fmaxf(red[2], red[3]));
  __syncthreads();
  float l = 0;
  for (int k = t; k < W; k += 256) { float p = expf(sc[k] - mx); sc[k] = p; l += p; }
  for (int of = 32; of >= 1; of >>= 1) l += __shfl_xor(l, of);
  if ((t & 63) == 0) red[t >> 6] = l;
  __syncthreads();
  l = red[0] + red[1] + red[2] + red[3];
  float inv = 1.f / l;
  for (int k = t; k < W; k += 256) sc[k] = rb(sc[k] * inv);
  __syncthreads();
  if (t < 128) {
    float a = 0;
    for (int k = 0; k < W; ++k) a += sc[k] * bf2f(vb[(size_t)k * DH + t]);
    o[b * 4096 + h * 128 + t] = rb(a);
  }
}
__global__ void k_o(const u16* x, const float* o, const u16* wo, float* h) {
  int n = blockIdx.x * 256 + threadIdx.x;
  float a0 = 0, a1 = 0;
  for (int k = 0; k < 4096; ++k) { float w = bf2f(wo[(size_t)k * 512 + n]); a0 += o[k] * w; a1 += o[4096 + k] * w; }
  h[n] = rb(bf2f(x[n]) + rb(a0)); h[512 + n] = rb(bf2f(x[512 + n]) + rb(a1));
}
__global__ void k_f1(const float* h, const u16* w1, float* u) {
  __shared__ float xr[2][512], xn[2][512], red[4];
  int t = threadIdx.x;
  for (int b = 0; b < 2; ++b) { xr[b][t] = h[b * 512 + t]; xr[b][t + 256] = h[b * 512 + t + 256]; }
  __syncthreads();
  block_ln(xr[0], xn[0], red); block_ln(xr[1], xn[1], red);
  int n = blockIdx.x * 256 + t;
  float a0 = 0, a1 = 0;
  for (int d = 0; d < 512; ++d) { float w = bf2f(w1[(size_t)d * F + n]); a0 += xn[0][d] * w; a1 += xn[1][d] * w; }
  a0 = rb(a0); a1 = rb(a1);
  u[n] = rb(a0 / (1.f + expf(-a0))); u[F + n] = rb(a1 / (1.f + expf(-a1)));
}
__global__ void k_f2(const float* h, const float* u, const u16* w2, u16* out) {
  int n = blockIdx.x * 256 + threadIdx.x;
  float a0 = 0, a1 = 0;
  for (int k = 0; k < F; ++k) { float w = bf2f(w2[(size_t)k * 512 + n]); a0 += u[k] * w; a1 += u[F + k] * w; }
  out[n] = f2bf(h[n] + rb(a0)); out[512 + n] = f2bf(h[512 + n] + rb(a1));
}

extern "C" void kernel_launch(void* const* d_in, const int* in_sizes, int n_in, void* d_out, int out_size, void* d_ws, size_t ws_size, hipStream_t stream) {
  const u16* x = (const u16*)d_in[0]; const u16* kc = (const u16*)d_in[1]; const u16* vc = (const u16*)d_in[2];
  const u16* wq = (const u16*)d_in[3]; const u16* wo = (const u16*)d_in[4];
  const u16* w1 = (const u16*)d_in[6]; const u16* w2 = (const u16*)d_in[7];
  float* ws = (float*)d_ws;
  float* q = ws; float* o = ws + 8192; float* h = ws + 16384; float* u = ws + 16384 + 1024;
  k_q<<<16, 256, 0, stream>>>(x, wq, q);
  k_attn<<<64, 256, 0, stream>>>(q, kc, vc, o);
  k_o<<<2, 256, 0, stream>>>(x, o, wo, h);
  k_f1<<<7, 256, 0, stream>>>(h, w1, u);
  k_f2<<<2, 256, 0, stream>>>(h, u, w2, (u16*)d_out);
}
```

```cpp
#include <hip/hip_runtime.h>
#include <hip/hip_bf16.h>

typedef unsigned short u16;
typedef unsigned int u32;
typedef unsigned long long u64;
typedef u32 v4u __attribute__((ext_vector_type(4)));
typedef u32 v2u __attribute__((ext_vector_type(2)));
typedef float v4f __attribute__((ext_vector_type(4)));
typedef float v2f __attribute__((ext_vector_type(2)));
typedef __bf16 bf8 __attribute__((ext_vector_type(8)));
typedef __bf16 bf2 __attribute__((ext_vector_type(2)));
typedef short s4 __attribute__((ext_vector_type(4)));
#define GAS __attribute__((address_space(1)))
#define LAS __attribute__((address_space(3)))
#define CAS __attribute__((address_space(4)))
#define DI __device__ __forceinline__

#define WO_START 250
#define WF_START 330
#define INFL 0
#define SCALE_RB 1

constexpr int SK = 8192, WIN = 2048, NF = 1792;
constexpr int NT = 576;
constexpr u32 EMPTY = 0xFFFFFFFFu;
constexpr int SPIN = 100000;
constexpr int WS_EPOCH = 0;
constexpr int WS_QG = 4096;
constexpr int WS_AF = WS_QG + 8 * 2 * 2 * 4096;
constexpr int WS_H2S = WS_AF + 128 * 128;
constexpr int WS_H2O = WS_H2S + 128 * 128;
constexpr int WS_H3 = WS_H2O + 128 * 1024;
constexpr int WS_H4 = WS_H3 + 16 * 1024;
constexpr int WS_END = WS_H4 + 56 * 4096;
constexpr int WS_DBG = 1 << 20;
constexpr int L_V = 0, L_K = 65536, L_QS = 131072, L_MX = L_QS + 4 * 272, L_P = L_MX + 128, L_AEND = L_P + 2048;
constexpr int L_WQ = 0, L_W2 = 32768, L_XN = 98304, L_RED = L_XN + 2048, L_C = L_RED + 4096, L_ST = L_C + 1024, L_U = L_ST + 128, L_WEND = L_U + 1024;
constexpr int LDS_BYTES = L_AEND > L_WEND ? L_AEND : L_WEND;

#define STAMP(i)
#define DUMP()

DI float asf(u32 x) { return __builtin_bit_cast(float, x); }
DI u32 asu(float x) { return __builtin_bit_cast(u32, x); }
DI u32 pk2(float lo, float hi) { v2f v = {lo, hi}; return __builtin_bit_cast(u32, __builtin_convertvector(v, bf2)); }
DI float lo16(u32 p) { return asf(p << 16); }
DI float hi16(u32 p) { return asf(p & 0xffff0000u); }
DI float rb(float x) { return lo16(pk2(x, x)); }
DI u32 canon(u32 x) { return x < 0xFFFFFFFEu ? x : 0xFFFFFFFEu; }
template <int CTRL> DI float dpp(float x) { return asf(__builtin_amdgcn_mov_dpp(asu(x), CTRL, 0xf, 0xf, true)); }
constexpr int XOR1 = 0xB1, XOR2 = 0x4E, HMIR = 0x141, ROR8 = 0x128, ROR4 = 0x124;
DI float sw16_sum(float x) { auto s = __builtin_amdgcn_permlane16_swap(asu(x), asu(x), false, false); return asf(s[0]) + asf(s[1]); }
DI float sw32_sum(float x) { auto s = __builtin_amdgcn_permlane32_swap(asu(x), asu(x), false, false); return asf(s[0]) + asf(s[1]); }
DI float sw16_max(float x) { auto s = __builtin_amdgcn_permlane16_swap(asu(x), asu(x), false, false); return fmaxf(asf(s[0]), asf(s[1])); }
DI float sw32_max(float x) { auto s = __builtin_amdgcn_permlane32_swap(asu(x), asu(x), false, false); return fmaxf(asf(s[0]), asf(s[1])); }
DI v4f mfma(v4u a, v4u b, v4f c) { return __builtin_amdgcn_mfma_f32_16x16x32_bf16(__builtin_bit_cast(bf8, a), __builtin_bit_cast(bf8, b), c, 0, 0, 0); }
DI float dot2(u32 a, u32 b, float c) { return __builtin_amdgcn_fdot2_f32_bf16(__builtin_bit_cast(bf2, a), __builtin_bit_cast(bf2, b), c, false); }
DI void dma16(const void* sbase, u32 voff, u32 lds_dst) {
  u32 keep;
  asm volatile("s_mov_b32 %0, m0\n\ts_mov_b32 m0, %3\n\ts_nop 0\n\tglobal_load_lds_dwordx4 %1, %2 nt\n\ts_mov_b32 m0, %0"
               : "=&s"(keep) : "v"(voff), "s"(sbase), "s"(lds_dst) : "memory");
}
#define VMWAIT(n) asm volatile("s_waitcnt vmcnt(" #n ")" ::: "memory")
DI void bar() { asm volatile("s_waitcnt lgkmcnt(0)\n\ts_barrier" ::: "memory"); }
DI v2u trd(const LAS char* p) { return __builtin_bit_cast(v2u, __builtin_amdgcn_ds_read_tr16_b64_v4i16((LAS s4*)p)); }
DI v4u trd2(const LAS char* p0, const LAS char* p1) { v2u a = trd(p0), b = trd(p1); return v4u{a.x, a.y, b.x, b.y}; }
DI bool full4(v4u a) { return (a.x != EMPTY) & (a.y != EMPTY) & (a.z != EMPTY) & (a.w != EMPTY); }
DI bool full2(v2u a) { return (a.x != EMPTY) & (a.y != EMPTY); }
DI void wait_until(u64 t) { for (int i = 0; i < 4000; ++i) { if (__builtin_amdgcn_s_memrealtime() >= t) break; __builtin_amdgcn_s_sleep(1); } }
#define LD16(off) __builtin_amdgcn_raw_buffer_load_b128(rs, (off), 0, 16)
#define LD8(off) __builtin_amdgcn_raw_buffer_load_b64(rs, (off), 0, 16)
#define ST16(v, off) __builtin_amdgcn_raw_buffer_store_b128((v), rs, (off), 0, 16)
#define ST8(v, off) __builtin_amdgcn_raw_buffer_store_b64((v), rs, (off), 0, 16)
#define ST4(v, off) __builtin_amdgcn_raw_buffer_store_b32((v), rs, (off), 0, 16)
#define LD4(off) __builtin_amdgcn_raw_buffer_load_b32(rs, (off), 0, 16)
#define MEMF() asm volatile("" ::: "memory")

__global__ void __launch_bounds__(NT, 3)
fused_block(const u16* x, const u16* kc, const u16* vc, const u16* wq, const u16* wo, const u16* w1, const u16* w2, u16* out, char* ws) {
  __shared__ __attribute__((aligned(16))) char smem[LDS_BYTES];
  LAS char* lds = (LAS char*)smem;
  const u32 lbase = (u32)(size_t)lds;
  const u64 t0 = __builtin_amdgcn_s_memrealtime();
  asm volatile("" : "+s"(x), "+s"(kc), "+s"(vc), "+s"(wq), "+s"(wo), "+s"(w1), "+s"(w2), "+s"(out), "+s"(ws));
  const int c = blockIdx.x, g = c & 7, idx = c >> 3;
  const int w = __builtin_amdgcn_readfirstlane(threadIdx.x >> 6), l = threadIdx.x & 63, r = l & 15, gm = l >> 4;
  const __amdgpu_buffer_rsrc_t rs = __builtin_amdgcn_make_buffer_rsrc(ws, 0, WS_END, 0x00020000);
  const v4u E4 = {EMPTY, EMPTY, EMPTY, EMPTY};
  constexpr float LOG2E = 1.4426950408889634f;

  if (idx < 16) {
    const int b = idx >> 3, s = idx & 7, rec = g * 16 + idx;
    const size_t kvoff = ((size_t)(b * 8 + g) * SK + (SK - WIN) + s * 256) * 256;
    if (w < 8) {
      const char* kb = (const char*)kc + kvoff; const char* vb = (const char*)vc + kvoff;
#pragma unroll
      for (int i = 0; i < 8; ++i) {
        const int row = 4 * i + gm;
        dma16(kb, (u32)((32 * w + row) * 256 + ((r ^ (row & 15)) * 16)), lbase + L_K + w * 8192 + i * 1024);
      }
      __builtin_amdgcn_s_barrier();
#pragma unroll
      for (int i = 0; i < 8; ++i) {
        const int row = 4 * i + gm;
        dma16(vb, (u32)((32 * w + row) * 256 + ((r ^ ((row & 7) << 1)) * 16)), lbase + L_V + w * 8192 + i * 1024);
      }
      STAMP(1);
      VMWAIT(8);
      STAMP(2);
      v4u kf[2][4];
#pragma unroll
      for (int t = 0; t < 2; ++t)
#pragma unroll
        for (int ks = 0; ks < 4; ++ks) kf[t][ks] = *(const LAS v4u*)(lds + L_K + w * 8192 + (16 * t + r) * 256 + (((4 * ks + gm) ^ r) * 16));
      bar();
      STAMP(3);
      v4f S0 = {0, 0, 0, 0}, S1 = {0, 0, 0, 0};
#pragma unroll
      for (int ks = 0; ks < 4; ++ks) {
        const v4u qf = *(const LAS v4u*)(lds + L_QS + (r & 3) * 272 + ks * 64 + gm * 16);
        S0 = mfma(kf[0][ks], qf, S0); S1 = mfma(kf[1][ks], qf, S1);
      }
      const int q4 = r >> 2;
      const bool hi = q4 & 2, od = q4 & 1;
      const float a0 = hi ? S1[0] : S0[0], a1 = hi ? S1[1] : S0[1], a2 = hi ? S1[2] : S0[2], a3 = hi ? S1[3] : S0[3];
      const float x0 = od ? a2 : a0, x1 = od ? a3 : a1;
      const float SC = SCALE_RB ? 0.08837890625f : 0.08838834764831845f;
      const u32 pa = pk2(x0, x1);
      const u32 pb = pk2(lo16(pa) * SC, hi16(pa) * SC);
      const float z0 = lo16(pb), z1 = hi16(pb);
      float m = fmaxf(z0, z1);
      m = fmaxf(m, dpp<ROR4>(m)); m = fmaxf(m, dpp<ROR8>(m)); m = sw16_max(m); m = sw32_max(m);
      if (l < 4) *(LAS float*)(lds + L_MX + l * 32 + w * 4) = m;
      bar();
      const v4f m0 = *(const LAS v4f*)(lds + L_MX + (r & 3) * 32), m1 = *(const LAS v4f*)(lds + L_MX + (r & 3) * 32 + 16);
      const float M = fmaxf(fmaxf(fmaxf(m0[0], m0[1]), fmaxf(m0[2], m0[3])), fmaxf(fmaxf(m1[0], m1[1]), fmaxf(m1[2], m1[3])));
      const float p0 = __builtin_amdgcn_exp2f((z0 - M) * LOG2E), p1 = __builtin_amdgcn_exp2f((z1 - M) * LOG2E);
      *(LAS u32*)(lds + L_P + w * 256 + gm * 64 + (r & 3) * 16 + q4 * 4) = pk2(p0, p1);
      STAMP(4);
      VMWAIT(0);
      STAMP(5);
      bar();
      STAMP(6);
      if (w < 4) {
        const int q = (l & 15) >> 2, p = l & 3;
        const int sx = 4 * (gm & 1) + q;
        const LAS char* v0 = lds + L_V + (4 * gm + q) * 256 + (((2 * w) ^ sx) * 32) + 8 * p;
        const LAS char* v1 = lds + L_V + (4 * gm + q) * 256 + (((2 * w + 1) ^ sx) * 32) + 8 * p;
        v4f A0 = {0, 0, 0, 0}, A1 = {0, 0, 0, 0};
#pragma unroll
        for (int k = 0; k < 8; ++k) {
          const v4u pf = *(const LAS v4u*)(lds + L_P + k * 256 + gm * 64 + (r & 3) * 16);
          A0 = mfma(trd2(v0 + k * 8192, v0 + k * 8192 + 4096), pf, A0);
          A1 = mfma(trd2(v1 + k * 8192, v1 + k * 8192 + 4096), pf, A1);
        }
        const v4f A = (q4 & 1) ? A1 : A0;
        const v2u o2 = {canon(pk2(A[0], A[1])), canon(pk2(A[2], A[3]))};
        if (r < 8) ST8(o2, WS_H2O + rec * 1024 + w * 256 + (r & 3) * 64 + (q4 & 1) * 32 + gm * 8);
      } else if (w == 4) {
        const v4u pa4 = *(const LAS v4u*)(lds + L_P + l * 16), pb4 = *(const LAS v4u*)(lds + L_P + 1024 + l * 16);
        const u32 ONE2 = 0x3F803F80u;
        float L = dot2(pa4.x, ONE2, 0.f); L = dot2(pa4.y, ONE2, L); L = dot2(pa4.z, ONE2, L); L = dot2(pa4.w, ONE2, L);
        float L2 = dot2(pb4.x, ONE2, 0.f); L2 = dot2(pb4.y, ONE2, L2); L2 = dot2(pb4.z, ONE2, L2); L2 = dot2(pb4.w, ONE2, L2);
        L += L2;
        L += dpp<ROR4>(L); L += dpp<ROR8>(L); L = sw16_sum(L); L = sw32_sum(L);
        const v2u st2 = {canon(asu(M)), canon(asu(L))};
        if (l < 4) ST8(st2, WS_H2S + rec * 128 + l * 8);
      }
      STAMP(7);
      DUMP();
      return;
    }
    __builtin_amdgcn_s_setprio(3);
    const u32 tag = LD4(WS_EPOCH) + 1u;
    ST16(E4, WS_H2O + rec * 1024 + l * 16);
    if (l < 2) ST16(E4, WS_H2S + rec * 128 + l * 16);
    __builtin_amdgcn_s_barrier();
    VMWAIT(0);
    if (l == 0) ST4(tag, WS_AF + rec * 128);
    STAMP(1);
    {
      const int qo = WS_QG + (g * 2 + b) * 8192 + l * 16;
      v4u qa[4], qb[4];
      for (int it = 0; it < SPIN; ++it) {
        MEMF();
#pragma unroll
        for (int i = 0; i < 4; ++i) { qa[i] = LD16(qo + i * 1024); qb[i] = LD16(qo + 4096 + i * 1024); }
        bool ok = true;
#pragma unroll
        for (int i = 0; i < 4; ++i) ok &= (qa[i].y == tag) & (qa[i].w == tag) & (qb[i].y == tag) & (qb[i].w == tag);
        if (__all(ok)) break;
      }
      STAMP(2);
#pragma unroll
      for (int i = 0; i < 4; ++i)
        *(LAS u32*)(lds + L_QS + i * 272 + l * 4) = pk2(asf(qa[i].x) + asf(qb[i].x), asf(qa[i].z) + asf(qb[i].z));
    }
    bar();
    if (idx >= 4) { DUMP(); return; }
    __builtin_amdgcn_s_barrier();
    __builtin_amdgcn_s_barrier();
    {
      const int rr = g * 4 + idx, rbat = rr >> 4, n0 = 32 * (rr & 15);
      const int grp = l >> 3, pc = l & 7;
      {
        const int o1 = WS_QG + (grp * 2) * 8192 + ((pc < 7) ? 4096 + pc * 512 : (n0 >> 6) * 512);
        for (int it = 0; it < SPIN; ++it) { MEMF(); const v2u t = LD8(o1); if (__all(t.y == tag)) break; }
      }
      STAMP(3);
      float h[4];
      {
        const int o3 = WS_H3 + (rbat * 8 + grp) * 1024 + n0 * 2 + pc * 8;
        v2u t;
        for (int it = 0; it < SPIN; ++it) { MEMF(); t = LD8(o3); if (__all(full2(t))) break; }
        h[0] = lo16(t.x); h[1] = hi16(t.x); h[2] = lo16(t.y); h[3] = hi16(t.y);
#pragma unroll
        for (int i = 0; i < 4; ++i) { h[i] += dpp<ROR8>(h[i]); h[i] = sw16_sum(h[i]); h[i] = rb(sw32_sum(h[i])); }
      }
      STAMP(4);
      const int o4 = WS_H4 + grp * 4096 + rbat * 2048 + n0 * 4 + pc * 16;
      v4u f[7];
      for (int it = 0; it < SPIN; ++it) {
        MEMF();
#pragma unroll
        for (int i = 0; i < 7; ++i) f[i] = LD16(o4 + i * 32768);
        bool ok = true;
#pragma unroll
        for (int i = 0; i < 7; ++i) ok &= full4(f[i]);
        if (__all(ok)) break;
      }
      STAMP(5);
      float y[4];
#pragma unroll
      for (int j = 0; j < 4; ++j) {
        float a = ((asf(f[0][j]) + asf(f[1][j])) + (asf(f[2][j]) + asf(f[3][j]))) + ((asf(f[4][j]) + asf(f[5][j])) + asf(f[6][j]));
        a += dpp<ROR8>(a); a = sw16_sum(a); a = sw32_sum(a);
        y[j] = h[j] + rb(a);
      }
      if (l < 8) *(GAS v2u*)(out + rbat * 512 + n0 + l * 4) = v2u{pk2(y[0], y[1]), pk2(y[2], y[3])};
      if (rr == 0 && l == 0) ST4(tag, WS_EPOCH);
      STAMP(6);
      DUMP();
      return;
    }
  }

  const int j = idx - 16, kh = j >> 3, cj = j & 7;
  const bool isO = kh == 0, isF = kh == 1 && cj < 7;
  const int fi = g * 7 + cj;
  const int q = (l & 15) >> 2, p = l & 3;
  if (w == 8) {
    const int b = l >> 5;
    const v4u xa = *(const GAS v4u*)(x + l * 16), xb = *(const GAS v4u*)(x + l * 16 + 8);
    const u32 tag = LD4(WS_EPOCH) + 1u;
    float v[16];
#pragma unroll
    for (int i = 0; i < 4; ++i) { v[2 * i] = lo16(xa[i]); v[2 * i + 1] = hi16(xa[i]); v[8 + 2 * i] = lo16(xb[i]); v[9 + 2 * i] = hi16(xb[i]); }
    float sm = 0;
#pragma unroll
    for (int i = 0; i < 16; ++i) sm += v[i];
    sm += dpp<XOR1>(sm); sm += dpp<XOR2>(sm); sm += dpp<ROR4>(sm); sm += dpp<ROR8>(sm); sm = sw16_sum(sm);
    const float mean = sm * (1.f / 512.f);
    float sq = 0;
#pragma unroll
    for (int i = 0; i < 16; ++i) { const float d = v[i] - mean; sq += d * d; }
    sq += dpp<XOR1>(sq); sq += dpp<XOR2>(sq); sq += dpp<ROR4>(sq); sq += dpp<ROR8>(sq); sq = sw16_sum(sq);
    const float mr = rb(mean), den = rb(sqrtf(rb(rb(sq * (1.f / 512.f)) + 1e-5f)));
    v4u na, nb;
#pragma unroll
    for (int i = 0; i < 4; ++i) {
      const u32 da = pk2(v[2 * i] - mr, v[2 * i + 1] - mr), db = pk2(v[8 + 2 * i] - mr, v[9 + 2 * i] - mr);
      na[i] = pk2(lo16(da) / den, hi16(da) / den); nb[i] = pk2(lo16(db) / den, hi16(db) / den);
    }
    (void)b;
    *(LAS v4u*)(lds + L_XN + l * 32) = na; *(LAS v4u*)(lds + L_XN + l * 32 + 16) = nb;
    STAMP(1);
    bar();
    __builtin_amdgcn_s_barrier();
    if (isO) {
      const int o1 = WS_AF + (g * 16 + r) * 128;
      for (int it = 0; it < SPIN; ++it) { MEMF(); const u32 t = LD4(o1); if (__all(t == tag)) break; }
    } else if (isF) {
      const int o1 = WS_QG + ((l >> 3) * 2) * 8192 + (l & 7) * 512;
      for (int it = 0; it < SPIN; ++it) { MEMF(); const v2u t = LD8(o1); if (__all(t.y == tag)) break; }
    }
    STAMP(2);
    if (isO || isF) __builtin_amdgcn_s_barrier();
    DUMP();
    return;
  }
  if (isO) { if (w == 0 && l < 16) ST16(E4, WS_H3 + ((l >> 3) * 8 + g) * 1024 + cj * 128 + (l & 7) * 16); }
  else if (isF) { if (w < 4) ST16(E4, WS_H4 + fi * 4096 + w * 1024 + l * 16); }
  {
    const char* wb = (const char*)wq + (size_t)(kh * 256 + w * 32) * 8192 + g * 1024 + cj * 128;
#pragma unroll
    for (int i = 0; i < 4; ++i) dma16(wb, (u32)((8 * i + (l >> 3)) * 8192 + (l & 7) * 16), lbase + L_WQ + w * 4096 + i * 1024);
  }
  const u32 tag = LD4(WS_EPOCH) + 1u;
  STAMP(1);
  VMWAIT(0);
  STAMP(2);
  {
    v4u bf[4];
    const LAS char* tb = lds + L_WQ + w * 4096 + (8 * gm + q) * 128 + 8 * p;
#pragma unroll
    for (int nt = 0; nt < 4; ++nt) bf[nt] = trd2(tb + nt * 32, tb + nt * 32 + 512);
    bar();
    const v4u af = *(const LAS v4u*)(lds + L_XN + (r & 1) * 1024 + (kh * 256 + w * 32 + 8 * gm) * 2);
    const v4f Z = {0, 0, 0, 0};
    const v4f D0 = mfma(af, bf[0], Z), D1 = mfma(af, bf[1], Z), D2 = mfma(af, bf[2], Z), D3 = mfma(af, bf[3], Z);
    const float e0 = (gm & 2) ? ((gm & 1) ? D3[0] : D2[0]) : ((gm & 1) ? D1[0] : D0[0]);
    const float e1 = (gm & 2) ? ((gm & 1) ? D3[1] : D2[1]) : ((gm & 1) ? D1[1] : D0[1]);
    *(LAS float*)(lds + L_RED + w * 512 + l * 4) = e0; *(LAS float*)(lds + L_RED + w * 512 + 256 + l * 4) = e1;
    bar();
    if (w < 2) {
      float a = 0;
#pragma unroll
      for (int i = 0; i < 8; ++i) a += *(const LAS float*)(lds + L_RED + i * 512 + w * 256 + l * 4);
      ST8((v2u{asu(a), tag}), WS_QG + (g * 2 + w) * 8192 + kh * 4096 + cj * 512 + l * 8);
    }
  }
  STAMP(3);
  if (isO) {
    wait_until(t0 + WO_START);
    {
      const char* wb = (const char*)wo + (size_t)(g * 512 + w * 64) * 1024 + cj * 128;
#pragma unroll
      for (int i = 0; i < 8; ++i) dma16(wb, (u32)((8 * i + (l >> 3)) * 1024 + (l & 7) * 16), lbase + L_W2 + w * 8192 + i * 1024);
    }
    float xres[2] = {0.f, 0.f};
    if (g == 0 && w == 0) { const u32 xx = *(const GAS u32*)(x + (l >> 5) * 512 + cj * 64 + (l & 31) * 2); xres[0] = lo16(xx); xres[1] = hi16(xx); }
    VMWAIT(0);
    STAMP(4);
    v4u bf[2][4];
    {
      const LAS char* tb = lds + L_W2 + w * 8192 + (8 * gm + q) * 128 + 8 * p;
#pragma unroll
      for (int ks = 0; ks < 2; ++ks)
#pragma unroll
        for (int nt = 0; nt < 4; ++nt) bf[ks][nt] = trd2(tb + ks * 4096 + nt * 32, tb + ks * 4096 + nt * 32 + 512);
    }
    bar();
    STAMP(5);
    const int h = w >> 1;
    const int oo = WS_H2O + (g * 16 + r) * 1024 + (w & 1) * 512 + h * 64 + gm * 16;
    const int os = WS_H2S + (g * 16 + 4 * gm) * 128 + h * 8;
    v4u a0, a1; v2u st[4];
    for (int it = 0; it < SPIN; ++it) {
      MEMF();
      a0 = LD16(oo); a1 = LD16(oo + 256);
#pragma unroll
      for (int i = 0; i < 4; ++i) st[i] = LD8(os + i * 128);
      const bool ok = full4(a0) & full4(a1) & full2(st[0]) & full2(st[1]) & full2(st[2]) & full2(st[3]);
      if (__all(ok)) break;
    }
    STAMP(6);
    v4f D[4];
#pragma unroll
    for (int nt = 0; nt < 4; ++nt) { D[nt] = mfma(a0, bf[0][nt], v4f{0, 0, 0, 0}); D[nt] = mfma(a1, bf[1][nt], D[nt]); }
    float M = fmaxf(fmaxf(asf(st[0].x), asf(st[1].x)), fmaxf(asf(st[2].x), asf(st[3].x)));
    M = sw16_max(M);
    float e[4], L = 0;
#pragma unroll
    for (int i = 0; i < 4; ++i) { e[i] = __builtin_amdgcn_exp2f((asf(st[i].x) - M) * LOG2E); L += e[i] * asf(st[i].y); }
    L = sw16_sum(L);
    const float inv = 1.f / L;
    float val[4];
#pragma unroll
    for (int nt = 0; nt < 4; ++nt) val[nt] = sw16_sum((e[0] * D[nt][0] + e[1] * D[nt][1]) + (e[2] * D[nt][2] + e[3] * D[nt][3])) * inv;
    {
      const float u0 = (gm & 1) ? val[2] : val[0], u1 = (gm & 1) ? val[3] : val[1];
      LAS float* rp = (LAS float*)(lds + L_RED + w * 512 + (gm >> 1) * 256 + (gm & 1) * 128 + r * 4);
      rp[0] = u0; rp[16] = u1;
    }
    bar();
    if (w == 0) {
      float s0 = xres[0], s1 = xres[1];
#pragma unroll
      for (int i = 0; i < 8; ++i) { const v2f t = *(const LAS v2f*)(lds + L_RED + i * 512 + l * 8); s0 += t[0]; s1 += t[1]; }
      ST4(canon(pk2(s0, s1)), WS_H3 + ((l >> 5) * 8 + g) * 1024 + cj * 128 + (l & 31) * 4);
    }
    STAMP(7);
    DUMP();
    return;
  }
  if (isF) {
    wait_until(t0 + WF_START);
    {
      const char* wa = (const char*)w1 + (size_t)(w * 64) * (NF * 2) + fi * 64;
#pragma unroll
      for (int i = 0; i < 4; ++i) dma16(wa, (u32)((16 * i + (l >> 2)) * (NF * 2) + (l & 3) * 16), lbase + L_W2 + w * 8192 + i * 1024);
      const char* wb = (const char*)w2 + (size_t)(fi * 32) * 1024 + w * 128;
#pragma unroll
      for (int i = 0; i < 4; ++i) dma16(wb, (u32)((8 * i + (l >> 3)) * 1024 + (l & 7) * 16), lbase + L_W2 + w * 8192 + 4096 + i * 1024);
    }
    VMWAIT(0);
    STAMP(4);
    v4u f1[2][2], f2[4];
    const v4u ONES = {0x3F803F80u, 0x3F803F80u, 0x3F803F80u, 0x3F803F80u};
    {
      const LAS char* ta = lds + L_W2 + w * 8192 + (8 * gm + q) * 64 + 8 * p;
#pragma unroll
      for (int ks = 0; ks < 2; ++ks)
#pragma unroll
        for (int ft = 0; ft < 2; ++ft) f1[ks][ft] = trd2(ta + ks * 2048 + ft * 32, ta + ks * 2048 + ft * 32 + 256);
      const LAS char* tb = lds + L_W2 + w * 8192 + 4096 + (8 * gm + q) * 128 + 8 * p;
#pragma unroll
      for (int nt = 0; nt < 4; ++nt) f2[nt] = trd2(tb + nt * 32, tb + nt * 32 + 512);
      v4f C0 = mfma(ONES, f1[0][0], v4f{0, 0, 0, 0}); C0 = mfma(ONES, f1[1][0], C0);
      v4f C1 = mfma(ONES, f1[0][1], v4f{0, 0, 0, 0}); C1 = mfma(ONES, f1[1][1], C1);
      if (l < 32) *(LAS float*)(lds + L_C + w * 128 + l * 4) = (gm & 1) ? C1[0] : C0[0];
    }
    bar();
    float csum = 0;
#pragma unroll
    for (int i = 0; i < 8; ++i) csum += *(const LAS float*)(lds + L_C + i * 128 + (l & 31) * 4);
    STAMP(5);
    const int oh = WS_H3 + r * 1024 + w * 128 + gm * 16;
    v4u a0, a1;
    for (int it = 0; it < SPIN; ++it) {
      MEMF();
      a0 = LD16(oh); a1 = LD16(oh + 64);
      if (__all(full4(a0) & full4(a1))) break;
    }
    STAMP(6);
    v4f Z0 = mfma(a0, f1[0][0], v4f{0, 0, 0, 0}), Z1 = mfma(a0, f1[0][1], v4f{0, 0, 0, 0});
    v4f G = mfma(a0, a0, v4f{0, 0, 0, 0}), S = mfma(a0, ONES, v4f{0, 0, 0, 0});
    Z0 = mfma(a1, f1[1][0], Z0); Z1 = mfma(a1, f1[1][1], Z1); G = mfma(a1, a1, G); S = mfma(a1, ONES, S);
    const float z0 = sw16_sum((Z0[0] + Z0[1]) + (Z0[2] + Z0[3])), z1 = sw16_sum((Z1[0] + Z1[1]) + (Z1[2] + Z1[3]));
    float gs = sw16_sum((G[0] + G[1]) + (G[2] + G[3]));
    const float ss = sw16_sum((S[0] + S[1]) + (S[2] + S[3]));
    gs += dpp<XOR1>(gs); gs += dpp<XOR2>(gs); gs += dpp<HMIR>(gs);
    *(LAS float*)(lds + L_RED + w * 256 + l * 4) = (gm & 1) ? z1 : z0;
    if (l == 0 || l == 40) *(LAS v2f*)(lds + L_ST + (l >> 5) * 64 + w * 8) = v2f{ss, gs};
    bar();
    float zr = 0, sm = 0, sq = 0;
#pragma unroll
    for (int i = 0; i < 8; ++i) zr += *(const LAS float*)(lds + L_RED + i * 256 + l * 4);
#pragma unroll
    for (int i = 0; i < 4; ++i) { const v4f t = *(const LAS v4f*)(lds + L_ST + (l >> 5) * 64 + i * 16); sm += t[0] + t[2]; sq += t[1] + t[3]; }
    const float mean = sm * (1.f / 512.f);
    const float var = fmaxf(sq * (1.f / 512.f) - mean * mean, 0.f);
    const float mr = rb(mean), den = rb(sqrtf(rb(rb(var) + 1e-5f)));
    const float z = rb((zr - mr * csum) / den);
    const float u = z / (1.f + __builtin_amdgcn_exp2f(-z * LOG2E));
    *(LAS u16*)(lds + L_U + w * 128 + l * 2) = (u16)pk2(u, u);
    MEMF();
    const v4u uf = *(const LAS v4u*)(lds + L_U + w * 128 + (r & 1) * 64 + gm * 16);
    const v4f Y0 = mfma(f2[0], uf, v4f{0, 0, 0, 0}), Y1 = mfma(f2[1], uf, v4f{0, 0, 0, 0});
    const v4f Y2 = mfma(f2[2], uf, v4f{0, 0, 0, 0}), Y3 = mfma(f2[3], uf, v4f{0, 0, 0, 0});
    const v4f Y = (r & 4) ? ((r & 2) ? Y3 : Y2) : ((r & 2) ? Y1 : Y0);
    const v4u yo = {canon(asu(Y[0])), canon(asu(Y[1])), canon(asu(Y[2])), canon(asu(Y[3]))};
    if (r < 8) ST16(yo, WS_H4 + fi * 4096 + (r & 1) * 2048 + (w * 64 + (r >> 1) * 16 + gm * 4) * 4);
    STAMP(7);
    DUMP();
    return;
  }
  DUMP();
}

extern "C" void kernel_launch(void* const* d_in, const int* in_sizes, int n_in, void* d_out, int out_size, void* d_ws, size_t ws_size,
                              hipStream_t stream) {
  fused_block<<<256, NT, 0, stream>>>((const u16*)d_in[0], (const u16*)d_in[1], (const u16*)d_in[2], (const u16*)d_in[3],
                                      (const u16*)d_in[4], (const u16*)d_in[6], (const u16*)d_in[7], (u16*)d_out, (char*)d_ws);
}
```

```cpp
#include <hip/hip_runtime.h>
#include <hip/hip_bf16.h>

typedef unsigned short u16;
typedef unsigned int u32;
typedef unsigned long long u64;
typedef u32 v4u __attribute__((ext_vector_type(4)));
typedef u32 v2u __attribute__((ext_vector_type(2)));
typedef float v4f __attribute__((ext_vector_type(4)));
typedef float v2f __attribute__((ext_vector_type(2)));
typedef __bf16 bf8 __attribute__((ext_vector_type(8)));
typedef __bf16 bf2 __attribute__((ext_vector_type(2)));
typedef short s4 __attribute__((ext_vector_type(4)));
#define GAS __attribute__((address_space(1)))
#define LAS __attribute__((address_space(3)))
#define DI __device__ __forceinline__

#define KDELAY 0
#define QSTART 200
#define WO_START 300
#define WF_START 380
#define FG_DELAY 60
#define INFL 0
#define H3REP 8
#define WPF 0
#define SCALE_RB 1

constexpr int SK = 8192, WIN = 2048, NF = 1792;
constexpr int NT = 576;
constexpr u32 EMPTY = 0xFFFFFFFFu;
constexpr int SPIN = 100000;
constexpr int WS_EPOCH = 0;
constexpr int WS_QG = 4096;
constexpr int WS_AF = WS_QG + 8 * 2 * 2 * 4096;
constexpr int WS_H2S = WS_AF + 128 * 128;
constexpr int WS_H2O = WS_H2S + 128 * 128;
constexpr int WS_H3 = WS_H2O + 128 * 1024;
constexpr int WS_H4 = WS_H3 + 8 * 16 * 1024;
constexpr int WS_END = WS_H4 + 56 * 4096;
constexpr int WS_DBG = 1 << 20;
constexpr int L_V = 0, L_K = 65536, L_QS = 131072, L_MX = L_QS + 4 * 272, L_P = L_MX + 128, L_TAG = L_P + 2048, L_AEND = L_TAG + 16;
constexpr int L_WQ = 0, L_W2 = 32768, L_RED = 98304, L_C = L_RED + 4096, L_ST = L_C + 1024, L_U = L_ST + 128, L_SC = L_U + 1024, L_FL = L_SC + 256,
              L_WEND = L_FL + 16;
constexpr int LDS_BYTES = L_AEND > L_WEND ? L_AEND : L_WEND;

#define STAMP(i)
#define DUMP()

DI float asf(u32 x) { return __builtin_bit_cast(float, x); }
DI u32 asu(float x) { return __builtin_bit_cast(u32, x); }
DI u32 pk2(float lo, float hi) { v2f v = {lo, hi}; return __builtin_bit_cast(u32, __builtin_convertvector(v, bf2)); }
DI float lo16(u32 p) { return asf(p << 16); }
DI float hi16(u32 p) { return asf(p & 0xffff0000u); }
DI float rb(float x) { return lo16(pk2(x, x)); }
DI u32 canon(u32 x) { return x < 0xFFFFFFFEu ? x : 0xFFFFFFFEu; }
template <int CTRL> DI float dpp(float x) { return asf(__builtin_amdgcn_mov_dpp(asu(x), CTRL, 0xf, 0xf, true)); }
constexpr int XOR1 = 0xB1, XOR2 = 0x4E, HMIR = 0x141, ROR8 = 0x128, ROR4 = 0x124;
DI float sw16_sum(float x) { auto s = __builtin_amdgcn_permlane16_swap(asu(x), asu(x), false, false); return asf(s[0]) + asf(s[1]); }
DI float sw32_sum(float x) { auto s = __builtin_amdgcn_permlane32_swap(asu(x), asu(x), false, false); return asf(s[0]) + asf(s[1]); }
DI float sw16_max(float x) { auto s = __builtin_amdgcn_permlane16_swap(asu(x), asu(x), false, false); return fmaxf(asf(s[0]), asf(s[1])); }
DI float sw32_max(float x) { auto s = __builtin_amdgcn_permlane32_swap(asu(x), asu(x), false, false); return fmaxf(asf(s[0]), asf(s[1])); }
DI v4f mfma(v4u a, v4u b, v4f c) { return __builtin_amdgcn_mfma_f32_16x16x32_bf16(__builtin_bit_cast(bf8, a), __builtin_bit_cast(bf8, b), c, 0, 0, 0); }
DI float dot2(u32 a, u32 b, float c) { return __builtin_amdgcn_fdot2_f32_bf16(__builtin_bit_cast(bf2, a), __builtin_bit_cast(bf2, b), c, false); }
DI void dma16(const void* sbase, u32 voff, u32 lds_dst) {
  u32 keep;
  asm volatile("s_mov_b32 %0, m0\n\ts_mov_b32 m0, %3\n\ts_nop 0\n\tglobal_load_lds_dwordx4 %1, %2 nt\n\ts_mov_b32 m0, %0"
               : "=&s"(keep) : "v"(voff), "s"(sbase), "s"(lds_dst) : "memory");
}
#define VMWAIT(n) asm volatile("s_waitcnt vmcnt(" #n ")" ::: "memory")
DI void bar() { asm volatile("s_waitcnt lgkmcnt(0)\n\ts_barrier" ::: "memory"); }
DI void rbar() { asm volatile("s_barrier" ::: "memory"); }
DI v2u trd(const LAS char* p) { return __builtin_bit_cast(v2u, __builtin_amdgcn_ds_read_tr16_b64_v4i16((LAS s4*)p)); }
DI v4u trd2(const LAS char* p0, const LAS char* p1) { v2u a = trd(p0), b = trd(p1); return v4u{a.x, a.y, b.x, b.y}; }
DI bool full4(v4u a) { return (a.x != EMPTY) & (a.y != EMPTY) & (a.z != EMPTY) & (a.w != EMPTY); }
DI bool full2(v2u a) { return (a.x != EMPTY) & (a.y != EMPTY); }
DI void wait_until(u64 t) {
#pragma unroll 1
  for (int i = 0; i < 4000; ++i) { if (__builtin_amdgcn_s_memrealtime() >= t) break; __builtin_amdgcn_s_sleep(1); }
}
#define LD16(off) __builtin_amdgcn_raw_buffer_load_b128(rs, (off), 0, 16)
#define LD8(off) __builtin_amdgcn_raw_buffer_load_b64(rs, (off), 0, 16)
#define LD4(off) __builtin_amdgcn_raw_buffer_load_b32(rs, (off), 0, 16)
#define ST16(v, off) __builtin_amdgcn_raw_buffer_store_b128((v), rs, (off), 0, 16)
#define ST8(v, off) __builtin_amdgcn_raw_buffer_store_b64((v), rs, (off), 0, 16)
#define ST4(v, off) __builtin_amdgcn_raw_buffer_store_b32((v), rs, (off), 0, 16)
#define MEMF() asm volatile("" ::: "memory")

__global__ void __launch_bounds__(NT, 3)
fused_block(const u16* x, const u16* kc, const u16* vc, const u16* wq, const u16* wo, const u16* w1, const u16* w2, u16* out, char* ws) {
  __shared__ __attribute__((aligned(16))) char smem[LDS_BYTES];
  LAS char* lds = (LAS char*)smem;
  const u32 lbase = (u32)(size_t)lds;
  const u64 t0 = __builtin_amdgcn_s_memrealtime();
  asm volatile("" : "+s"(x), "+s"(kc), "+s"(vc), "+s"(wq), "+s"(wo), "+s"(w1), "+s"(w2), "+s"(out), "+s"(ws));
  const int c = blockIdx.x, g = c & 7, idx = c >> 3;
  const int w = __builtin_amdgcn_readfirstlane(threadIdx.x >> 6), l = threadIdx.x & 63, r = l & 15, gm = l >> 4;
  const __amdgpu_buffer_rsrc_t rs = __builtin_amdgcn_make_buffer_rsrc(ws, 0, WS_END, 0x00020000);
  const v4u E4 = {EMPTY, EMPTY, EMPTY, EMPTY};
  constexpr float LOG2E = 1.4426950408889634f;
  constexpr u32 ONE2 = 0x3F803F80u;
  const int rep = H3REP == 8 ? g : 0;

  if (idx < 16) {
    const int b = idx >> 3, s = idx & 7, rec = g * 16 + idx;
    const size_t kvoff = ((size_t)(b * 8 + g) * SK + (SK - WIN) + s * 256) * 256;
    if (w < 8) {
      const char* kb = (const char*)kc + kvoff; const char* vb = (const char*)vc + kvoff;
      STAMP(11);
#pragma unroll
      for (int i = 0; i < 8; ++i) {
        const int row = 4 * i + gm;
        dma16(kb, (u32)((32 * w + row) * 256 + ((r ^ (row & 15)) * 16)), lbase + L_K + w * 8192 + i * 1024);
      }
      rbar();
#pragma unroll
      for (int i = 0; i < 8; ++i) {
        const int row = 4 * i + gm;
        dma16(vb, (u32)((32 * w + row) * 256 + ((r ^ ((row & 7) << 1)) * 16)), lbase + L_V + w * 8192 + i * 1024);
      }
      STAMP(1);
      VMWAIT(8);
      v4u kf[2][4];
#pragma unroll
      for (int t = 0; t < 2; ++t)
#pragma unroll
        for (int ks = 0; ks < 4; ++ks) kf[t][ks] = *(const LAS v4u*)(lds + L_K + w * 8192 + (16 * t + r) * 256 + (((4 * ks + gm) ^ r) * 16));
      bar();
      STAMP(2);
      v4f S0 = {0, 0, 0, 0}, S1 = {0, 0, 0, 0};
#pragma unroll
      for (int ks = 0; ks < 4; ++ks) {
        const v4u qf = *(const LAS v4u*)(lds + L_QS + (r & 3) * 272 + ks * 64 + gm * 16);
        S0 = mfma(kf[0][ks], qf, S0); S1 = mfma(kf[1][ks], qf, S1);
      }
      const int q4 = r >> 2;
      const bool hi = q4 & 2, od = q4 & 1;
      const float a0 = hi ? S1[0] : S0[0], a1 = hi ? S1[1] : S0[1], a2 = hi ? S1[2] : S0[2], a3 = hi ? S1[3] : S0[3];
      const float x0 = od ? a2 : a0, x1 = od ? a3 : a1;
      const float SC = SCALE_RB ? 0.08837890625f : 0.08838834764831845f;
      const u32 pa = pk2(x0, x1);
      const u32 pb = pk2(lo16(pa) * SC, hi16(pa) * SC);
      const float z0 = lo16(pb), z1 = hi16(pb);
      float m = fmaxf(z0, z1);
      m = fmaxf(m, dpp<ROR4>(m)); m = fmaxf(m, dpp<ROR8>(m)); m = sw16_max(m); m = sw32_max(m);
      if (l < 4) *(LAS float*)(lds + L_MX + l * 32 + w * 4) = m;
      bar();
      const v4f m0 = *(const LAS v4f*)(lds + L_MX + (r & 3) * 32), m1 = *(const LAS v4f*)(lds + L_MX + (r & 3) * 32 + 16);
      const float M = fmaxf(fmaxf(fmaxf(m0[0], m0[1]), fmaxf(m0[2], m0[3])), fmaxf(fmaxf(m1[0], m1[1]), fmaxf(m1[2], m1[3])));
      const float p0 = __builtin_amdgcn_exp2f((z0 - M) * LOG2E), p1 = __builtin_amdgcn_exp2f((z1 - M) * LOG2E);
      *(LAS u32*)(lds + L_P + w * 256 + gm * 64 + (r & 3) * 16 + q4 * 4) = pk2(p0, p1);
      bar();
      STAMP(3);
      if (w < 4) {
        v4u pf[8];
#pragma unroll
        for (int k = 0; k < 8; ++k) pf[k] = *(const LAS v4u*)(lds + L_P + k * 256 + gm * 64 + (r & 3) * 16);
        const int q = (l & 15) >> 2, p = l & 3;
        const int sx = 4 * (gm & 1) + q;
        const LAS char* v0 = lds + L_V + (4 * gm + q) * 256 + (((2 * w) ^ sx) * 32) + 8 * p;
        const LAS char* v1 = lds + L_V + (4 * gm + q) * 256 + (((2 * w + 1) ^ sx) * 32) + 8 * p;
        VMWAIT(0);
        STAMP(4);
        rbar();
        STAMP(5);
        v4f A0 = {0, 0, 0, 0}, A1 = {0, 0, 0, 0};
#pragma unroll
        for (int k = 0; k < 8; ++k) {
          A0 = mfma(trd2(v0 + k * 8192, v0 + k * 8192 + 4096), pf[k], A0);
          A1 = mfma(trd2(v1 + k * 8192, v1 + k * 8192 + 4096), pf[k], A1);
        }
        const v4f A = (q4 & 1) ? A1 : A0;
        const v2u o2 = {canon(pk2(A[0], A[1])), canon(pk2(A[2], A[3]))};
        if (r < 8) ST8(o2, WS_H2O + rec * 1024 + w * 256 + (r & 3) * 64 + (q4 & 1) * 32 + gm * 8);
        STAMP(6);
        DUMP();
        return;
      }
      VMWAIT(0);
      STAMP(4);
      rbar();
      if (w < 7 || idx >= 4) { DUMP(); return; }
      {
        const u32 tag = *(const LAS u32*)(lds + L_TAG);
        const int rr = g * 4 + idx, rbat = rr >> 4, n0 = 32 * (rr & 15);
        const int grp = l >> 3, pc = l & 7;
        {
          const int o1 = WS_QG + (grp * 2) * 8192 + ((pc < 7) ? 4096 + pc * 512 : (n0 >> 6) * 512);
#pragma unroll 1
          for (int it = 0; it < SPIN; ++it) { MEMF(); const v2u t = LD8(o1); if (__all(t.y == tag)) break; }
        }
        float h[4];
        {
          const int o3 = WS_H3 + rep * 16384 + (rbat * 8 + grp) * 1024 + n0 * 2 + pc * 8;
          v2u t;
#pragma unroll 1
          for (int it = 0; it < SPIN; ++it) { MEMF(); t = LD8(o3); if (__all(full2(t))) break; __builtin_amdgcn_s_sleep(2); }
          h[0] = lo16(t.x); h[1] = hi16(t.x); h[2] = lo16(t.y); h[3] = hi16(t.y);
#pragma unroll
          for (int i = 0; i < 4; ++i) { h[i] += dpp<ROR8>(h[i]); h[i] = sw16_sum(h[i]); h[i] = rb(sw32_sum(h[i])); }
        }
        STAMP(8);
        const int o4 = WS_H4 + grp * 4096 + rbat * 2048 + n0 * 4 + pc * 16;
        v4u f[7];
#pragma unroll 1
        for (int it = 0; it < SPIN; ++it) {
          MEMF();
#pragma unroll
          for (int i = 0; i < 7; ++i) f[i] = LD16(o4 + i * 32768);
          bool ok = true;
#pragma unroll
          for (int i = 0; i < 7; ++i) ok &= full4(f[i]);
          if (__all(ok)) break;
        }
        STAMP(9);
        float y[4];
#pragma unroll
        for (int j = 0; j < 4; ++j) {
          float a = ((asf(f[0][j]) + asf(f[1][j])) + (asf(f[2][j]) + asf(f[3][j]))) + ((asf(f[4][j]) + asf(f[5][j])) + asf(f[6][j]));
          a += dpp<ROR8>(a); a = sw16_sum(a); a = sw32_sum(a);
          y[j] = h[j] + rb(a);
        }
        if (l < 8) *(GAS v2u*)(out + rbat * 512 + n0 + l * 4) = v2u{pk2(y[0], y[1]), pk2(y[2], y[3])};
        if (rr == 0 && l == 0) ST4(tag, WS_EPOCH);
        STAMP(10);
        DUMP();
        return;
      }
    }
    __builtin_amdgcn_s_setprio(3);
    const u32 tag = LD4(WS_EPOCH) + 1u;
    ST16(E4, WS_H2O + rec * 1024 + l * 16);
    if (l < 2) ST16(E4, WS_H2S + rec * 128 + l * 16);
    rbar();
    VMWAIT(0);
    if (l == 0) { ST4(tag, WS_AF + rec * 128); *(LAS u32*)(lds + L_TAG) = tag; }
    STAMP(1);
    wait_until(t0 + QSTART);
    {
      const int qo = WS_QG + (g * 2 + b) * 8192 + l * 16;
      v4u qa[4], qb[4];
#pragma unroll 1
      for (int it = 0; it < SPIN; ++it) {
        MEMF();
#pragma unroll
        for (int i = 0; i < 4; ++i) { qa[i] = LD16(qo + i * 1024); qb[i] = LD16(qo + 4096 + i * 1024); }
        bool ok = true;
#pragma unroll
        for (int i = 0; i < 4; ++i) ok &= (qa[i].y == tag) & (qa[i].w == tag) & (qb[i].y == tag) & (qb[i].w == tag);
        if (__all(ok)) break;
      }
      STAMP(2);
#pragma unroll
      for (int i = 0; i < 4; ++i)
        *(LAS u32*)(lds + L_QS + i * 272 + l * 4) = pk2(asf(qa[i].x) + asf(qb[i].x), asf(qa[i].z) + asf(qb[i].z));
    }
    bar();
    rbar();
    const v4f m0 = *(const LAS v4f*)(lds + L_MX + (r & 3) * 32), m1 = *(const LAS v4f*)(lds + L_MX + (r & 3) * 32 + 16);
    const float M = fmaxf(fmaxf(fmaxf(m0[0], m0[1]), fmaxf(m0[2], m0[3])), fmaxf(fmaxf(m1[0], m1[1]), fmaxf(m1[2], m1[3])));
    rbar();
    {
      const v4u pa4 = *(const LAS v4u*)(lds + L_P + l * 16), pb4 = *(const LAS v4u*)(lds + L_P + 1024 + l * 16);
      float L = dot2(pa4.x, ONE2, 0.f); L = dot2(pa4.y, ONE2, L); L = dot2(pa4.z, ONE2, L); L = dot2(pa4.w, ONE2, L);
      float L2 = dot2(pb4.x, ONE2, 0.f); L2 = dot2(pb4.y, ONE2, L2); L2 = dot2(pb4.z, ONE2, L2); L2 = dot2(pb4.w, ONE2, L2);
      L += L2;
      L += dpp<ROR4>(L); L += dpp<ROR8>(L); L = sw16_sum(L); L = sw32_sum(L);
      const v2u st2 = {canon(asu(M)), canon(asu(L))};
      if (l < 4) ST8(st2, WS_H2S + rec * 128 + l * 8);
    }
    STAMP(3);
    DUMP();
    return;
  }

  const int j = idx - 16, kh = j >> 3, cj = j & 7;
  const bool isO = kh == 0, isF = kh == 1 && cj < 7;
  const int fi = g * 7 + cj;
  const int q = (l & 15) >> 2, p = l & 3;
  if (w == 8) {
    const u32 tag = LD4(WS_EPOCH) + 1u;
    if (isO) {
      if (H3REP == 8) {
#pragma unroll
        for (int i = 0; i < 2; ++i) ST16(E4, WS_H3 + (l >> 3) * 16384 + (i * 8 + g) * 1024 + cj * 128 + (l & 7) * 16);
      } else if (l < 16) ST16(E4, WS_H3 + ((l >> 3) * 8 + g) * 1024 + cj * 128 + (l & 7) * 16);
    } else if (isF) {
#pragma unroll
      for (int i = 0; i < 4; ++i) ST16(E4, WS_H4 + fi * 4096 + i * 1024 + l * 16);
    }
    if (l == 0) *(LAS v2u*)(lds + L_FL) = v2u{0u, tag};
    VMWAIT(0);
    STAMP(1);
    bar();
    if (isO) {
      const int o1 = WS_AF + (g * 16 + r) * 128;
#pragma unroll 1
      for (int it = 0; it < SPIN; ++it) { MEMF(); const u32 t = LD4(o1); if (__all(t == tag)) break; __builtin_amdgcn_s_sleep(2); }
      STAMP(2);
      const int os = WS_H2S + (g * 16 + (l >> 2)) * 128 + (l & 3) * 8;
      v2u st;
#pragma unroll 1
      for (int it = 0; it < SPIN; ++it) { MEMF(); st = LD8(os); if (__any(full2(st))) break; __builtin_amdgcn_s_sleep(2); }
      STAMP(3);
      rbar();
#pragma unroll 1
      for (int it = 0; it < SPIN; ++it) { if (__all(full2(st))) break; MEMF(); st = LD8(os); }
      STAMP(4);
      const float m = asf(st.x);
      float M = fmaxf(m, dpp<ROR4>(m)); M = fmaxf(M, dpp<ROR8>(M)); M = sw16_max(M);
      const float e = __builtin_amdgcn_exp2f((m - M) * LOG2E);
      float L = e * asf(st.y);
      L += dpp<ROR4>(L); L += dpp<ROR8>(L); L = sw16_sum(L);
      *(LAS float*)(lds + L_SC + (l & 3) * 64 + (l >> 2) * 4) = e * __builtin_amdgcn_rcpf(L);
      MEMF();
      if (l == 0) *(volatile LAS u32*)(lds + L_FL) = 1u;
      STAMP(5);
    } else if (isF) {
      const int o1 = WS_QG + ((l >> 3) * 2) * 8192 + (l & 7) * 512;
#pragma unroll 1
      for (int it = 0; it < SPIN; ++it) { MEMF(); const v2u t = LD8(o1); if (__all(t.y == tag)) break; __builtin_amdgcn_s_sleep(2); }
      STAMP(2);
      const int os = WS_H2S + (g * 16 + (l & 15)) * 128;
#pragma unroll 1
      for (int it = 0; it < 2000; ++it) { MEMF(); const u32 t = LD4(os); if (__any(t != EMPTY)) break; __builtin_amdgcn_s_sleep(4); }
      STAMP(3);
      wait_until(__builtin_amdgcn_s_memrealtime() + FG_DELAY);
      rbar();
    }
    DUMP();
    return;
  }
  {
    v4u xa, xb, xf;
    asm volatile("global_load_dwordx4 %0, %3, %5\n\tglobal_load_dwordx4 %1, %3, %5 offset:16\n\tglobal_load_dwordx4 %2, %4, %5"
                 : "=&v"(xa), "=&v"(xb), "=&v"(xf)
                 : "v"(l * 32), "v"((r & 1) * 1024 + (kh * 256 + w * 32 + 8 * gm) * 2), "s"(x) : "memory");
    const char* wb = (const char*)wq + (size_t)(kh * 256 + w * 32) * 8192 + g * 1024 + cj * 128;
#pragma unroll
    for (int i = 0; i < 4; ++i) dma16(wb, (u32)((8 * i + (l >> 3)) * 8192 + (l & 7) * 16), lbase + L_WQ + w * 4096 + i * 1024);
    STAMP(1);
    asm volatile("s_waitcnt vmcnt(4)" : "+v"(xa), "+v"(xb), "+v"(xf) :: "memory");
    float s0 = dot2(xa.x, ONE2, 0.f), s1 = dot2(xb.x, ONE2, 0.f);
    s0 = dot2(xa.y, ONE2, s0); s1 = dot2(xb.y, ONE2, s1); s0 = dot2(xa.z, ONE2, s0); s1 = dot2(xb.z, ONE2, s1);
    s0 = dot2(xa.w, ONE2, s0); s1 = dot2(xb.w, ONE2, s1);
    float sm = s0 + s1;
    sm += dpp<XOR1>(sm); sm += dpp<XOR2>(sm); sm += dpp<ROR4>(sm); sm += dpp<ROR8>(sm); sm = sw16_sum(sm);
    const float mean = sm * (1.f / 512.f);
    float q0 = 0, q1 = 0;
#pragma unroll
    for (int i = 0; i < 4; ++i) {
      const float d0 = lo16(xa[i]) - mean, d1 = hi16(xa[i]) - mean, d2 = lo16(xb[i]) - mean, d3 = hi16(xb[i]) - mean;
      q0 = fmaf(d0, d0, q0); q1 = fmaf(d1, d1, q1); q0 = fmaf(d2, d2, q0); q1 = fmaf(d3, d3, q1);
    }
    float sq = q0 + q1;
    sq += dpp<XOR1>(sq); sq += dpp<XOR2>(sq); sq += dpp<ROR4>(sq); sq += dpp<ROR8>(sq); sq = sw16_sum(sq);
    float mr = rb(mean), rd = __builtin_amdgcn_rcpf(rb(__builtin_amdgcn_sqrtf(rb(rb(sq * (1.f / 512.f)) + 1e-5f))));
    {
      auto a = __builtin_amdgcn_permlane32_swap(asu(mr), asu(mr), false, false);
      auto d = __builtin_amdgcn_permlane32_swap(asu(rd), asu(rd), false, false);
      mr = asf((r & 1) ? a[1] : a[0]); rd = asf((r & 1) ? d[1] : d[0]);
    }
    v4u af;
#pragma unroll
    for (int i = 0; i < 4; ++i) { const u32 d = pk2(lo16(xf[i]) - mr, hi16(xf[i]) - mr); af[i] = pk2(lo16(d) * rd, hi16(d) * rd); }
    asm volatile("" : "+v"(af));
    STAMP(2);
    VMWAIT(0);
    STAMP(3);
    v4u bf[4];
    const LAS char* tb = lds + L_WQ + w * 4096 + (8 * gm + q) * 128 + 8 * p;
#pragma unroll
    for (int nt = 0; nt < 4; ++nt) bf[nt] = trd2(tb + nt * 32, tb + nt * 32 + 512);
    const v4f Z = {0, 0, 0, 0};
    const v4f D0 = mfma(af, bf[0], Z), D1 = mfma(af, bf[1], Z), D2 = mfma(af, bf[2], Z), D3 = mfma(af, bf[3], Z);
    const float e0 = (gm & 2) ? ((gm & 1) ? D3[0] : D2[0]) : ((gm & 1) ? D1[0] : D0[0]);
    const float e1 = (gm & 2) ? ((gm & 1) ? D3[1] : D2[1]) : ((gm & 1) ? D1[1] : D0[1]);
    *(LAS float*)(lds + L_RED + w * 512 + l * 4) = e0; *(LAS float*)(lds + L_RED + w * 512 + 256 + l * 4) = e1;
    bar();
    if (w < 2) {
      float t[8];
      const u32 tag = *(const LAS u32*)(lds + L_FL + 4);
#pragma unroll
      for (int i = 0; i < 8; ++i) t[i] = *(const LAS float*)(lds + L_RED + i * 512 + w * 256 + l * 4);
      const float a = ((t[0] + t[1]) + (t[2] + t[3])) + ((t[4] + t[5]) + (t[6] + t[7]));
      ST8((v2u{asu(a), tag}), WS_QG + (g * 2 + w) * 8192 + kh * 4096 + cj * 512 + l * 8);
    }
  }
  STAMP(4);
  if (isO) {
    wait_until(t0 + WO_START);
    {
      const char* wb = (const char*)wo + (size_t)(g * 512 + w * 64) * 1024 + cj * 128;
#pragma unroll
      for (int i = 0; i < 8; ++i) dma16(wb, (u32)((8 * i + (l >> 3)) * 1024 + (l & 7) * 16), lbase + L_W2 + w * 8192 + i * 1024);
    }
    float xres[2] = {0.f, 0.f};
    if (g == 0) { const u32 xx = *(const GAS u32*)(x + (l >> 5) * 512 + cj * 64 + (l & 31) * 2); xres[0] = lo16(xx); xres[1] = hi16(xx); }
    VMWAIT(0);
    STAMP(5);
    v4u bf[2][4];
    {
      const LAS char* tb = lds + L_W2 + w * 8192 + (8 * gm + q) * 128 + 8 * p;
#pragma unroll
      for (int ks = 0; ks < 2; ++ks)
#pragma unroll
        for (int nt = 0; nt < 4; ++nt) bf[ks][nt] = trd2(tb + ks * 4096 + nt * 32, tb + ks * 4096 + nt * 32 + 512);
    }
    bar();
    STAMP(6);
    const int h = w >> 1;
    const int oo = WS_H2O + (g * 16 + r) * 1024 + (w & 1) * 512 + h * 64 + gm * 16;
    v4u a0, a1;
#pragma unroll 1
    for (int it = 0; it < SPIN; ++it) {
      MEMF();
      a0 = LD16(oo); a1 = LD16(oo + 256);
      if (__all(full4(a0) & full4(a1))) break;
    }
    STAMP(7);
    v4f D[4];
#pragma unroll
    for (int nt = 0; nt < 4; ++nt) { D[nt] = mfma(a0, bf[0][nt], v4f{0, 0, 0, 0}); D[nt] = mfma(a1, bf[1][nt], D[nt]); }
    v4f sc;
#pragma unroll 1
    for (int it = 0; it < 4 * SPIN; ++it) {
      const u32 fl = *(const volatile LAS u32*)(lds + L_FL);
      sc = *(const volatile LAS v4f*)(lds + L_SC + h * 64 + gm * 16);
      if (fl) break;
    }
    float val[4];
#pragma unroll
    for (int nt = 0; nt < 4; ++nt) val[nt] = (sc[0] * D[nt][0] + sc[1] * D[nt][1]) + (sc[2] * D[nt][2] + sc[3] * D[nt][3]);
    {
      auto sa = __builtin_amdgcn_permlane16_swap(asu(val[0]), asu(val[2]), false, false);
      auto sb = __builtin_amdgcn_permlane16_swap(asu(val[1]), asu(val[3]), false, false);
      const float u0 = asf(sa[0]) + asf(sa[1]), u1 = asf(sb[0]) + asf(sb[1]);
      LAS float* rp = (LAS float*)(lds + L_RED + w * 512 + (gm >> 1) * 256 + (gm & 1) * 128 + r * 4);
      rp[0] = u0; rp[16] = u1;
    }
    bar();
    if (H3REP == 8 || w == 0) {
      v2f t[8];
#pragma unroll
      for (int i = 0; i < 8; ++i) t[i] = *(const LAS v2f*)(lds + L_RED + i * 512 + l * 8);
      const float s0 = (((t[0][0] + t[1][0]) + (t[2][0] + t[3][0])) + ((t[4][0] + t[5][0]) + (t[6][0] + t[7][0]))) + xres[0];
      const float s1 = (((t[0][1] + t[1][1]) + (t[2][1] + t[3][1])) + ((t[4][1] + t[5][1]) + (t[6][1] + t[7][1]))) + xres[1];
      ST4(canon(pk2(s0, s1)), WS_H3 + w * 16384 + ((l >> 5) * 8 + g) * 1024 + cj * 128 + (l & 31) * 4);
    }
    STAMP(8);
    DUMP();
    return;
  }
  if (isF) {
    wait_until(t0 + WF_START);
    {
      const char* wa = (const char*)w1 + (size_t)(w * 64) * (NF * 2) + fi * 64;
#pragma unroll
      for (int i = 0; i < 4; ++i) dma16(wa, (u32)((16 * i + (l >> 2)) * (NF * 2) + (l & 3) * 16), lbase + L_W2 + w * 8192 + i * 1024);
      const char* wb = (const char*)w2 + (size_t)(fi * 32) * 1024 + w * 128;
#pragma unroll
      for (int i = 0; i < 4; ++i) dma16(wb, (u32)((8 * i + (l >> 3)) * 1024 + (l & 7) * 16), lbase + L_W2 + w * 8192 + 4096 + i * 1024);
    }
    VMWAIT(0);
    STAMP(5);
    v4u f1[2][2], f2[4];
    const v4u ONES = {ONE2, ONE2, ONE2, ONE2};
    {
      const LAS char* ta = lds + L_W2 + w * 8192 + (8 * gm + q) * 64 + 8 * p;
#pragma unroll
      for (int ks = 0; ks < 2; ++ks)
#pragma unroll
        for (int ft = 0; ft < 2; ++ft) f1[ks][ft] = trd2(ta + ks * 2048 + ft * 32, ta + ks * 2048 + ft * 32 + 256);
      const LAS char* tb = lds + L_W2 + w * 8192 + 4096 + (8 * gm + q) * 128 + 8 * p;
#pragma unroll
      for (int nt = 0; nt < 4; ++nt) f2[nt] = trd2(tb + nt * 32, tb + nt * 32 + 512);
      v4f C0 = mfma(ONES, f1[0][0], v4f{0, 0, 0, 0}); C0 = mfma(ONES, f1[1][0], C0);
      v4f C1 = mfma(ONES, f1[0][1], v4f{0, 0, 0, 0}); C1 = mfma(ONES, f1[1][1], C1);
      if (l < 32) *(LAS float*)(lds + L_C + w * 128 + l * 4) = (gm & 1) ? C1[0] : C0[0];
    }
    bar();
    float csum = 0;
#pragma unroll
    for (int i = 0; i < 8; ++i) csum += *(const LAS float*)(lds + L_C + i * 128 + (l & 31) * 4);
    STAMP(6);
    const int oh = WS_H3 + rep * 16384 + r * 1024 + w * 128 + gm * 16;
    v4u a0, a1;
#pragma unroll 1
    for (int it = 0; it < SPIN; ++it) {
      MEMF();
      a0 = LD16(oh); a1 = LD16(oh + 64);
      if (__all(full4(a0) & full4(a1))) break;
    }
    STAMP(7);
    v4f G = mfma(a0, a0, v4f{0, 0, 0, 0}), S = mfma(a0, ONES, v4f{0, 0, 0, 0});
    G = mfma(a1, a1, G); S = mfma(a1, ONES, S);
    v4f Z0 = mfma(a0, f1[0][0], v4f{0, 0, 0, 0}), Z1 = mfma(a0, f1[0][1], v4f{0, 0, 0, 0});
    Z0 = mfma(a1, f1[1][0], Z0); Z1 = mfma(a1, f1[1][1], Z1);
    float gs = sw16_sum((G[0] + G[1]) + (G[2] + G[3]));
    const float ss = sw16_sum((S[0] + S[1]) + (S[2] + S[3]));
    gs += dpp<XOR1>(gs); gs += dpp<XOR2>(gs); gs += dpp<HMIR>(gs);
    if (l == 0 || l == 40) *(LAS v2f*)(lds + L_ST + (l >> 5) * 64 + w * 8) = v2f{ss, gs};
    {
      const float za = (Z0[0] + Z0[1]) + (Z0[2] + Z0[3]), zb = (Z1[0] + Z1[1]) + (Z1[2] + Z1[3]);
      auto s = __builtin_amdgcn_permlane16_swap(asu(za), asu(zb), false, false);
      *(LAS float*)(lds + L_RED + w * 256 + l * 4) = asf(s[0]) + asf(s[1]);
    }
    bar();
    float zt[8]; v4f tt[4];
#pragma unroll
    for (int i = 0; i < 4; ++i) tt[i] = *(const LAS v4f*)(lds + L_ST + (l >> 5) * 64 + i * 16);
#pragma unroll
    for (int i = 0; i < 8; ++i) zt[i] = *(const LAS float*)(lds + L_RED + i * 256 + l * 4);
    const float sm = ((tt[0][0] + tt[0][2]) + (tt[1][0] + tt[1][2])) + ((tt[2][0] + tt[2][2]) + (tt[3][0] + tt[3][2]));
    const float sq = ((tt[0][1] + tt[0][3]) + (tt[1][1] + tt[1][3])) + ((tt[2][1] + tt[2][3]) + (tt[3][1] + tt[3][3]));
    const float zr = ((zt[0] + zt[1]) + (zt[2] + zt[3])) + ((zt[4] + zt[5]) + (zt[6] + zt[7]));
    const float mean = sm * (1.f / 512.f);
    const float var = fmaxf(sq * (1.f / 512.f) - mean * mean, 0.f);
    const float mr = rb(mean), rd = __builtin_amdgcn_rcpf(rb(__builtin_amdgcn_sqrtf(rb(rb(var) + 1e-5f))));
    const float z = rb((zr - mr * csum) * rd);
    const float u = z * __builtin_amdgcn_rcpf(1.f + __builtin_amdgcn_exp2f(-z * LOG2E));
    *(LAS u16*)(lds + L_U + w * 128 + l * 2) = (u16)pk2(u, u);
    MEMF();
    const v4u uf = *(const LAS v4u*)(lds + L_U + w * 128 + (r & 1) * 64 + gm * 16);
    const v4f Y0 = mfma(f2[0], uf, v4f{0, 0, 0, 0}), Y1 = mfma(f2[1], uf, v4f{0, 0, 0, 0});
    const v4f Y2 = mfma(f2[2], uf, v4f{0, 0, 0, 0}), Y3 = mfma(f2[3], uf, v4f{0, 0, 0, 0});
    const v4f Y = (r & 4) ? ((r & 2) ? Y3 : Y2) : ((r & 2) ? Y1 : Y0);
    const v4u yo = {canon(asu(Y[0])), canon(asu(Y[1])), canon(asu(Y[2])), canon(asu(Y[3]))};
    if (r < 8) ST16(yo, WS_H4 + fi * 4096 + (r & 1) * 2048 + (w * 64 + (r >> 1) * 16 + gm * 4) * 4);
    STAMP(8);
    DUMP();
    return;
  }
  DUMP();
}

extern "C" void kernel_launch(void* const* d_in, const int* in_sizes, int n_in, void* d_out, int out_size, void* d_ws, size_t ws_size,
                              hipStream_t stream) {
  fused_block<<<256, NT, 0, stream>>>((const u16*)d_in[0], (const u16*)d_in[1], (const u16*)d_in[2], (const u16*)d_in[3],
                                      (const u16*)d_in[4], (const u16*)d_in[6], (const u16*)d_in[7], (u16*)d_out, (char*)d_ws);
}
```

```cpp
#include <hip/hip_runtime.h>
#include <hip/hip_bf16.h>

typedef unsigned short u16;
typedef unsigned int u32;
typedef unsigned long long u64;
typedef u32 v4u __attribute__((ext_vector_type(4)));
typedef u32 v2u __attribute__((ext_vector_type(2)));
typedef float v4f __attribute__((ext_vector_type(4)));
typedef float v2f __attribute__((ext_vector_type(2)));
typedef __bf16 bf8 __attribute__((ext_vector_type(8)));
typedef __bf16 bf2 __attribute__((ext_vector_type(2)));
typedef short s4 __attribute__((ext_vector_type(4)));
typedef u64 v8q __attribute__((ext_vector_type(8)));
#define GAS __attribute__((address_space(1)))
#define LAS __attribute__((address_space(3)))
#define DI __device__ __forceinline__

#define KDELAY 140
#define QSTART 270
#define Q2 60
#define Q3 130
#define VSTART 280
#define POLL_O 420
#define POLL_F 500
#define POLL_E 300
#define WO_START 300
#define WO3_EARLY 100
#define WO7_EARLY 30
#define WF_START 360
#define WF3_EARLY 40
#define FDELTA 90
#define RDELTA 225
#define OSLEEP 1
#define HSLEEP 4
#define CODE_BYTES 16752
#define KPRIO 2
#define GK 4
#define KEYOFF(t) ((((t) / GK) * (8 * GK) + (t) % GK) * 256)
#define NPV 2
#define H3REP 8
#define SCALE_RB 1

constexpr int SK = 8192, WIN = 2048, NF = 1792;
#ifndef NVL
#define NVL 2
#endif
#define NWAVES (10 + NVL)
constexpr int NT = 64 * NWAVES;
constexpr u32 EMPTY = 0xFFFFFFFFu;
constexpr int SPIN = 100000;
constexpr int WS_EPOCH = 0;
constexpr int WS_QG = 4096;
constexpr int WS_WF = WS_QG + 8 * 2 * 2 * 4096;
constexpr int WS_OT = WS_WF + 8 * 128;
constexpr int WS_H2S = WS_OT + 64 * 128;
constexpr int WS_H2O = WS_H2S + 128 * 128;
constexpr int WS_H3 = WS_H2O + 128 * 1024;
constexpr int WS_H4 = WS_H3 + 8 * 16 * 1024;
constexpr int WS_END = WS_H4 + 56 * 4096;
constexpr int L_QS = 0, L_MX = L_QS + 4 * 272, L_P = L_MX + 128, L_TAG = L_P + 2048, L_QF = L_TAG + 16, L_V = 4096, L_K = L_V + 65536, L_AEND = L_K + 65536;
constexpr int L_RQ = 0, L_RED = 4096, L_C = 8192, L_ST = L_C + 1024, L_DUM = L_ST + 128, L_U = L_DUM + 512, L_H = L_U + 1024, L_SC = L_H + 2048,
              L_FL = L_SC + 256, L_WQ = 16384, L_W2 = L_WQ + 32768, L_WEND = L_W2 + 65536;
constexpr int LDS_BYTES = L_AEND > L_WEND ? L_AEND : L_WEND;

DI float asf(u32 x) { return __builtin_bit_cast(float, x); }
DI u32 asu(float x) { return __builtin_bit_cast(u32, x); }
DI u32 pk2(float lo, float hi) { v2f v = {lo, hi}; return __builtin_bit_cast(u32, __builtin_convertvector(v, bf2)); }
DI float lo16(u32 p) { return asf(p << 16); }
DI float hi16(u32 p) { return asf(p & 0xffff0000u); }
DI float rb(float x) { return lo16(pk2(x, x)); }
DI u32 canon(u32 x) { return x < 0xFFFFFFFEu ? x : 0xFFFFFFFEu; }
template <int CTRL> DI float dpp(float x) { return asf(__builtin_amdgcn_mov_dpp(asu(x), CTRL, 0xf, 0xf, true)); }
constexpr int XOR1 = 0xB1, XOR2 = 0x4E, HMIR = 0x141, ROR8 = 0x128, ROR4 = 0x124;
DI float sw16_sum(float x) { auto s = __builtin_amdgcn_permlane16_swap(asu(x), asu(x), false, false); return asf(s[0]) + asf(s[1]); }
DI float sw32_sum(float x) { auto s = __builtin_amdgcn_permlane32_swap(asu(x), asu(x), false, false); return asf(s[0]) + asf(s[1]); }
DI float sw16_max(float x) { auto s = __builtin_amdgcn_permlane16_swap(asu(x), asu(x), false, false); return fmaxf(asf(s[0]), asf(s[1])); }
DI float sw32_max(float x) { auto s = __builtin_amdgcn_permlane32_swap(asu(x), asu(x), false, false); return fmaxf(asf(s[0]), asf(s[1])); }
DI int wave_imax(int x) {
  x = max(x, __builtin_amdgcn_mov_dpp(x, XOR1, 0xf, 0xf, true)); x = max(x, __builtin_amdgcn_mov_dpp(x, XOR2, 0xf, 0xf, true));
  x = max(x, __builtin_amdgcn_mov_dpp(x, ROR4, 0xf, 0xf, true)); x = max(x, __builtin_amdgcn_mov_dpp(x, ROR8, 0xf, 0xf, true));
  auto a = __builtin_amdgcn_permlane16_swap(x, x, false, false); x = max((int)a[0], (int)a[1]);
  auto c = __builtin_amdgcn_permlane32_swap(x, x, false, false); x = max((int)c[0], (int)c[1]);
  return __builtin_amdgcn_readfirstlane(x);
}
DI bool lane_of(u64 m) { return __builtin_amdgcn_inverse_ballot_w64(m); }
DI v4f mfma(v4u a, v4u b, v4f c) { return __builtin_amdgcn_mfma_f32_16x16x32_bf16(__builtin_bit_cast(bf8, a), __builtin_bit_cast(bf8, b), c, 0, 0, 0); }
DI float dot2(u32 a, u32 b, float c) { return __builtin_amdgcn_fdot2_f32_bf16(__builtin_bit_cast(bf2, a), __builtin_bit_cast(bf2, b), c, false); }
DI void dma16(const void* sbase, u32 voff, u32 lds_dst) {
  u32 keep;
  asm volatile("s_mov_b32 %0, m0\n\ts_mov_b32 m0, %3\n\ts_nop 0\n\tglobal_load_lds_dwordx4 %1, %2 nt\n\ts_mov_b32 m0, %0"
               : "=&s"(keep) : "v"(voff), "s"(sbase), "s"(lds_dst) : "memory");
}
#define VMWAIT(n) asm volatile("s_waitcnt vmcnt(" #n ")" ::: "memory")
DI void bar() { asm volatile("s_waitcnt lgkmcnt(0)\n\ts_barrier" ::: "memory"); }
DI void rbar() { asm volatile("s_barrier" ::: "memory"); }
DI v2u trd(const LAS char* p) { return __builtin_bit_cast(v2u, __builtin_amdgcn_ds_read_tr16_b64_v4i16((LAS s4*)p)); }
DI v4u trd2(const LAS char* p0, const LAS char* p1) { v2u a = trd(p0), b = trd(p1); return v4u{a.x, a.y, b.x, b.y}; }
DI bool full4(v4u a) { return (a.x != EMPTY) & (a.y != EMPTY) & (a.z != EMPTY) & (a.w != EMPTY); }
DI bool full2(v2u a) { return (a.x != EMPTY) & (a.y != EMPTY); }
DI u32 max3u(u32 a, u32 b, u32 c) { return max(max(a, b), c); }
DI bool none(bool x) { return __builtin_amdgcn_ballot_w64(x) == 0; }
#define PIN(x) asm volatile("" : "+v"(x))
#define PINO(x) do { asm volatile("" : "+v"(x)); __builtin_assume(x >= 0 && x < (1 << 24)); } while (0)
#define PINS(x) asm volatile("" : "+s"(x))
DI void wait_until(u64 t) {
#pragma unroll 1
  for (int i = 0; i < 4000; ++i) { if (__builtin_amdgcn_s_memrealtime() >= t) break; __builtin_amdgcn_s_sleep(1); }
}
#define LD16(off) __builtin_amdgcn_raw_buffer_load_b128(rs, (off), 0, 16)
#define LD8(off) __builtin_amdgcn_raw_buffer_load_b64(rs, (off), 0, 16)
#define LD4(off) __builtin_amdgcn_raw_buffer_load_b32(rs, (off), 0, 16)
#define ST16(v, off) __builtin_amdgcn_raw_buffer_store_b128((v), rs, (off), 0, 16)
#define ST8(v, off) __builtin_amdgcn_raw_buffer_store_b64((v), rs, (off), 0, 16)
#define ST4(v, off) __builtin_amdgcn_raw_buffer_store_b32((v), rs, (off), 0, 16)
#define MEMF() asm volatile("" ::: "memory")
#define SB() __builtin_amdgcn_sched_barrier(0)

__global__ void __launch_bounds__(NT, (NWAVES + 3) / 4)
fused_block(const u16* x_, const u16* kc_, const u16* vc_, const u16* wq_, const u16* wo_, const u16* w1_, const u16* w2_, char* ws_, u16* out) {
  __shared__ __attribute__((aligned(16))) char smem[LDS_BYTES];
  LAS char* lds = (LAS char*)smem;
  const u32 lbase = (u32)(size_t)lds;
  u64 t0;
  v8q ka;
  {
    u64 pc; u32 ta, tb, keep, vo;
    asm volatile("s_load_dwordx16 %0, %7, 0x0\n\ts_memrealtime %1\n\t"
                 "v_readfirstlane_b32 %3, %9\n\ts_lshr_b32 %4, %8, 3\n\ts_cmp_gt_u32 %4, 15\n\ts_cselect_b32 %4, 0x100000, %4\n\t"
                 "s_lshr_b32 %3, %3, 6\n\ts_sub_u32 %3, %3, 9\n\ts_lshl_b32 %5, %3, 4\n\ts_add_u32 %4, %4, %5\n\t"
                 "s_cmp_lt_u32 %4, %10\n\ts_cbranch_scc0 1f\n\t"
                 "s_getpc_b64 %2\n\ts_andn2_b64 %2, %2, 0xff\n\ts_lshl_b32 %4, %4, 10\n\tv_and_b32 %6, 63, %9\n\t"
                 "s_mul_i32 %3, %3, %12\n\ts_add_u32 %3, %3, %11\n\tv_lshl_or_b32 %6, %6, 4, %4\n\tv_min_u32 %6, %13, %6\n\t"
                 "s_mov_b32 %5, m0\n\ts_mov_b32 m0, %3\n\ts_nop 0\n\tglobal_load_lds_dwordx4 %6, %2\n\ts_mov_b32 m0, %5\n"
                 "1:\n\ts_waitcnt lgkmcnt(0)"
                 : "=&s"(ka), "=&s"(t0), "=&s"(pc), "=&s"(ta), "=&s"(tb), "=&s"(keep), "=&v"(vo)
                 : "s"(__builtin_amdgcn_kernarg_segment_ptr()), "s"(blockIdx.x), "v"(threadIdx.x), "i"((CODE_BYTES + 1023) / 1024),
                   "s"(lbase + L_V), "i"(8192 / NVL), "i"((CODE_BYTES & ~15) - 16) : "memory", "scc");
  }
  const u16 *x = (const u16*)ka[0], *kc = (const u16*)ka[1], *vc = (const u16*)ka[2], *wq = (const u16*)ka[3], *wo = (const u16*)ka[4],
            *w1 = (const u16*)ka[5], *w2 = (const u16*)ka[6];
  char* ws = (char*)ka[7];
  const int c = blockIdx.x, g = c & 7, idx = c >> 3;
  const int w = __builtin_amdgcn_readfirstlane(threadIdx.x >> 6), l = threadIdx.x & 63, r = l & 15, gm = l >> 4;
  const __amdgpu_buffer_rsrc_t rs = __builtin_amdgcn_make_buffer_rsrc(ws, 0, WS_END, 0x00020000);
  const v4u E4 = {EMPTY, EMPTY, EMPTY, EMPTY};
  constexpr float LOG2E = 1.4426950408889634f;
  constexpr u32 ONE2 = 0x3F803F80u;
  const int rep = H3REP == 8 ? g : 0;

  auto hint = [&](u32 tag) {
    v2u t;
#pragma unroll 1
    for (int it = 0; it < 1000; ++it) { MEMF(); t = LD8(WS_OT + g * 1024 + (l & 7) * 128); if (none(t.y != tag)) break; __builtin_amdgcn_s_sleep(HSLEEP); }
    return t0 + (u64)(u32)min(max(wave_imax((int)(t.x - (u32)t0)), 0), 3000);
  };

  if (idx < 16) {
    const int b = idx >> 3, s = idx & 7, rec = g * 16 + idx;
    const size_t kvoff = ((size_t)(b * 8 + g) * SK + (SK - WIN) + s * GK) * 256;
    if (w == 0) {
      if (l < 32) *(LAS u32*)(lds + L_MX + l * 4) = EMPTY;
      if (l == 32) *(LAS v2u*)(lds + L_QF) = v2u{0u, 0u};
    }
    bar();
    if (w > 8 && w < 9 + NVL) {
      const char* vb = (const char*)vc + kvoff;
      constexpr int PB = 8 / NVL;
      const int v = w - 9;
      wait_until(t0 + VSTART);
#pragma unroll
      for (int i = 0; i < 8 * PB; ++i) {
        const int pc = v * PB + (i % PB), row = 4 * pc + gm;
        dma16(vb, (u32)(KEYOFF(32 * (i / PB) + row) + ((r ^ ((row & 7) << 1)) * 16)), lbase + L_V + (i / PB) * 8192 + pc * 1024);
      }
      asm volatile("s_waitcnt vmcnt(%0)\n\ts_barrier" :: "i"(NPV * PB) : "memory");
      VMWAIT(0);
      rbar();
      return;
    }
    if (w < 8) {
      const char* kb = (const char*)kc + kvoff;
      wait_until(t0 + KDELAY);
      __builtin_amdgcn_s_setprio(KPRIO);
#pragma unroll
      for (int i = 0; i < 8; ++i) {
        const int row = 4 * i + gm;
        dma16(kb, (u32)(KEYOFF(32 * w + row) + ((r ^ (row & 15)) * 16)), lbase + L_K + w * 8192 + i * 1024);
      }
      VMWAIT(0);
      v4u kf[2][4];
#pragma unroll
      for (int t = 0; t < 2; ++t)
#pragma unroll
        for (int ks = 0; ks < 4; ++ks) kf[t][ks] = *(const LAS v4u*)(lds + L_K + w * 8192 + (16 * t + r) * 256 + (((4 * ks + gm) ^ r) * 16));
      v4u qf[4];
#pragma unroll 1
      for (int it = 0; it < 8 * SPIN; ++it) {
        const u32 fl = *(const volatile LAS u32*)(lds + L_QF);
#pragma unroll
        for (int ks = 0; ks < 4; ++ks) qf[ks] = *(const volatile LAS v4u*)(lds + L_QS + (r & 3) * 272 + ks * 64 + gm * 16);
        if (__builtin_amdgcn_readfirstlane(fl)) break;
        __builtin_amdgcn_s_sleep(1);
      }
      v4f S0 = {0, 0, 0, 0}, S1 = {0, 0, 0, 0};
#pragma unroll
      for (int ks = 0; ks < 4; ++ks) { S0 = mfma(kf[0][ks], qf[ks], S0); S1 = mfma(kf[1][ks], qf[ks], S1); }
      const int q4 = r >> 2;
      const bool hi = q4 & 2, od = q4 & 1;
      const float a0 = hi ? S1[0] : S0[0], a1 = hi ? S1[1] : S0[1], a2 = hi ? S1[2] : S0[2], a3 = hi ? S1[3] : S0[3];
      const float x0 = od ? a2 : a0, x1 = od ? a3 : a1;
      const float SC = SCALE_RB ? 0.08837890625f : 0.08838834764831845f;
      const u32 pa = pk2(x0, x1);
      const u32 pb = pk2(lo16(pa) * SC, hi16(pa) * SC);
      const float z0 = lo16(pb), z1 = hi16(pb);
      float m = fmaxf(z0, z1);
      m = fmaxf(m, dpp<ROR4>(m)); m = fmaxf(m, dpp<ROR8>(m)); m = sw16_max(m); m = sw32_max(m);
      if (l < 4) *(volatile LAS u32*)(lds + L_MX + l * 32 + w * 4) = canon(asu(m));
      v4u m0, m1;
#pragma unroll 1
      for (int it = 0; it < 8 * SPIN; ++it) {
        m0 = *(const volatile LAS v4u*)(lds + L_MX + (r & 3) * 32); m1 = *(const volatile LAS v4u*)(lds + L_MX + (r & 3) * 32 + 16);
        if (none(max3u(max3u(m0.x, m0.y, m0.z), max3u(m0.w, m1.x, m1.y), max(m1.z, m1.w)) == EMPTY)) break;
      }
      const float M = fmaxf(fmaxf(fmaxf(asf(m0.x), asf(m0.y)), fmaxf(asf(m0.z), asf(m0.w))), fmaxf(fmaxf(asf(m1.x), asf(m1.y)), fmaxf(asf(m1.z), asf(m1.w))));
      const float p0 = __builtin_amdgcn_exp2f((z0 - M) * LOG2E), p1 = __builtin_amdgcn_exp2f((z1 - M) * LOG2E);
      *(LAS u32*)(lds + L_P + w * 256 + gm * 64 + (r & 3) * 16 + q4 * 4) = pk2(p0, p1);
      {
        const int q = (l & 15) >> 2, p = l & 3;
        const int sx = 4 * (gm & 1) + q;
        const LAS char* v0 = lds + L_V + (4 * gm + q) * 256 + ((((2 * w) & 7) ^ sx) * 32) + 8 * p;
        const LAS char* v1 = lds + L_V + (4 * gm + q) * 256 + ((((2 * w + 1) & 7) ^ sx) * 32) + 8 * p;
        int so = WS_H2O + rec * 1024 + gm * 256 + w * 64 + r * 4;
        const LAS char* pp = lds + L_P + gm * 64 + (r >> 2) * 16;
        PIN(v0); PIN(v1); PIN(so); PIN(pp);
        asm volatile("s_waitcnt lgkmcnt(0)\n\ts_barrier\n\ts_cmp_lt_u32 %0, 4\n\ts_cbranch_scc1 1f\n\ts_barrier\n1:"
                     :: "s"(w) : "memory", "scc");
        v4f A0 = {0, 0, 0, 0}, A1 = {0, 0, 0, 0};
        if (__builtin_expect(w < 4, 1)) {
          v4u pf[8];
#pragma unroll
          for (int k = 0; k < 8; ++k) pf[k] = *(const LAS v4u*)(pp + k * 256);
#pragma unroll
          for (int k = 0; k < 8 - NPV; ++k) {
            A0 = mfma(pf[k], trd2(v0 + k * 8192, v0 + k * 8192 + 4096), A0);
            A1 = mfma(pf[k], trd2(v1 + k * 8192, v1 + k * 8192 + 4096), A1);
          }
          PIN(A0); PIN(A1);
#pragma unroll
          for (int k = 8 - NPV; k < 8; ++k) PIN(pf[k]);
          rbar();
#pragma unroll
          for (int k = 8 - NPV; k < 8; ++k) {
            A0 = mfma(pf[k], trd2(v0 + k * 8192, v0 + k * 8192 + 4096), A0);
            A1 = mfma(pf[k], trd2(v1 + k * 8192, v1 + k * 8192 + 4096), A1);
          }
          ST4(canon(pk2(A0[0], A1[0])), so);
          return;
        }
      }
      if (w < 7 || idx >= 4) return;
      {
        const u32 tag = *(const LAS u32*)(lds + L_TAG);
        const int rr = g * 4 + idx, n0 = 16 * rr;
        const int grp = l >> 3, pc = l & 7;
        PINS(out);
        u64 tgo;
        {
          const int o1 = WS_WF + grp * 128 + ((pc < 7) ? 8 + pc : (n0 >> 6)) * 4;
#pragma unroll 1
          for (int it = 0; it < SPIN; ++it) { MEMF(); const u32 t = LD4(o1); if (none(t != tag)) break; }
          tgo = hint(tag);
          wait_until(tgo + FDELTA + 10);
        }
        float h[4];
        {
          const int o3 = WS_H3 + rep * 16384 + grp * 1024 + n0 * 2 + pc * 4;
          u32 t0_, t1_;
#pragma unroll 1
          for (int it = 0; it < SPIN; ++it) {
            MEMF(); t0_ = LD4(o3); t1_ = LD4(o3 + 8192);
            if (none(max(t0_, t1_) == EMPTY)) break;
            __builtin_amdgcn_s_sleep(2);
          }
          h[0] = lo16(t0_); h[1] = lo16(t1_); h[2] = hi16(t0_); h[3] = hi16(t1_);
#pragma unroll
          for (int i = 0; i < 4; ++i) { h[i] += dpp<ROR8>(h[i]); h[i] = sw16_sum(h[i]); h[i] = rb(sw32_sum(h[i])); }
        }
        float ha = (l & 8) ? h[1] : h[0], hb = (l & 8) ? h[3] : h[2];
        int oa = ((l >> 3) & 1) * 1024 + (n0 + 2 * pc) * 2;
        PIN(ha); PIN(hb); PIN(oa);
        wait_until(tgo + RDELTA);
        const int o4 = WS_H4 + grp * 4096 + n0 * 8 + pc * 16;
        v4u f[7];
#pragma unroll 1
        for (int it = 0; it < SPIN; ++it) {
          MEMF();
#pragma unroll
          for (int i = 0; i < 7; ++i) f[i] = LD16(o4 + i * 32768);
          u32 m = 0;
#pragma unroll
          for (int i = 0; i < 7; ++i) m = max3u(max3u(m, f[i].x, f[i].y), f[i].z, f[i].w);
          if (none(m == EMPTY)) break;
        }
        float y[4];
#pragma unroll
        for (int j = 0; j < 4; ++j)
          y[j] = ((asf(f[0][j]) + asf(f[1][j])) + (asf(f[2][j]) + asf(f[3][j]))) + ((asf(f[4][j]) + asf(f[5][j])) + asf(f[6][j]));
        float ya = ((l & 8) ? y[1] : y[0]) + dpp<ROR8>((l & 8) ? y[0] : y[1]);
        float yb = ((l & 8) ? y[3] : y[2]) + dpp<ROR8>((l & 8) ? y[2] : y[3]);
        ya = sw32_sum(sw16_sum(ya)); yb = sw32_sum(sw16_sum(yb));
        const u32 yr = pk2(ya, yb);
        if (l < 16) *(GAS u32*)((GAS char*)out + oa) = pk2(ha + lo16(yr), hb + hi16(yr));
        if (rr == 0 && l == 0) ST4(tag, WS_EPOCH);
        return;
      }
    }
    __builtin_amdgcn_s_setprio(3);
    const bool hlp = w > 8;
    const u32 tag = LD4(WS_EPOCH) + 1u;
    ST16(E4, hlp ? WS_END : WS_H2O + rec * 1024 + l * 16);
    VMWAIT(0);
    if (l == 0) *(LAS u32*)(lds + L_TAG) = tag;
    v4u qa[8], qb[8];
    {
      const int qo = WS_QG + (g * 2 + b) * 8192 + l * 16;
      wait_until(t0 + (hlp ? QSTART + Q2 : QSTART));
#pragma unroll
      for (int i = 0; i < 8; ++i) qa[i] = LD16(qo + i * 1024);
      MEMF();
      int qo2 = WS_END;
      if (hlp) {
        wait_until(t0 + QSTART + Q3);
        if (__builtin_amdgcn_readfirstlane(*(const volatile LAS u32*)(lds + L_QF))) return;
        qo2 = qo;
      }
#pragma unroll
      for (int i = 0; i < 8; ++i) qb[i] = LD16(qo2 + i * 1024);
      MEMF();
      const u32 tA = tag & 15u, tB = ((tag >> 4) ^ tag) & 15u, tC = ((tag >> 8) ^ tag) & 15u, tD = ((tag >> 12) ^ tag) & 15u;
      auto put = [&](const v4u* q) {
        v4u e[8];
        u32 x0 = 0;
#pragma unroll
        for (int i = 0; i < 8; ++i) {
          e[i] = v4u{q[i].x ^ tA, q[i].y ^ tB, q[i].z ^ tC, q[i].w ^ tD};
          x0 |= (e[i].x | e[i].y) | (e[i].z | e[i].w);
        }
        if (!none((x0 & 15u) != 0)) return false;
#pragma unroll
        for (int i = 0; i < 2; ++i) {
          float t[4];
#pragma unroll
          for (int k = 0; k < 4; ++k) t[k] = (asf(e[i][k]) + asf(e[2 + i][k])) + (asf(e[4 + i][k]) + asf(e[6 + i][k]));
          *(LAS v2u*)(lds + L_QS + (2 * i + (l >> 5)) * 272 + (l & 31) * 8) = v2u{pk2(t[0], t[1]), pk2(t[2], t[3])};
        }
        return true;
      };
      if (hlp) {
        const bool ok = put(qa) || put(qb);
        MEMF();
        if (l == 0) *(volatile LAS u32*)(lds + (ok ? L_QF : L_QF + 4)) = 1u;
        return;
      }
      if (!put(qa)) {
        u32 f0 = 0;
#pragma unroll 1
        for (int it = 0; it < 8 * SPIN; ++it) {
          const v2u f = *(const volatile LAS v2u*)(lds + L_QF);
          f0 = __builtin_amdgcn_readfirstlane(f.x);
          if (f0 | __builtin_amdgcn_readfirstlane(f.y)) break;
          __builtin_amdgcn_s_sleep(1);
        }
#pragma unroll 1
        for (int it = 0; it < (f0 ? 0 : SPIN); ++it) {
          MEMF();
#pragma unroll
          for (int i = 0; i < 8; ++i) qa[i] = LD16(qo + i * 1024);
          if (put(qa)) break;
        }
      }
      MEMF();
      if (l == 0) *(volatile LAS u32*)(lds + L_QF) = 1u;
    }
    bar();
    {
      const v4f m0 = *(const LAS v4f*)(lds + L_MX + (r & 3) * 32), m1 = *(const LAS v4f*)(lds + L_MX + (r & 3) * 32 + 16);
      const float M = fmaxf(fmaxf(fmaxf(m0[0], m0[1]), fmaxf(m0[2], m0[3])), fmaxf(fmaxf(m1[0], m1[1]), fmaxf(m1[2], m1[3])));
      const v4u pa4 = *(const LAS v4u*)(lds + L_P + l * 16), pb4 = *(const LAS v4u*)(lds + L_P + 1024 + l * 16);
      float L = dot2(pa4.x, ONE2, 0.f); L = dot2(pa4.y, ONE2, L); L = dot2(pa4.z, ONE2, L); L = dot2(pa4.w, ONE2, L);
      float L2 = dot2(pb4.x, ONE2, 0.f); L2 = dot2(pb4.y, ONE2, L2); L2 = dot2(pb4.z, ONE2, L2); L2 = dot2(pb4.w, ONE2, L2);
      L += L2;
      L += dpp<ROR4>(L); L += dpp<ROR8>(L); L = sw16_sum(L); L = sw32_sum(L);
      v4u sv = {asu(M), tag, asu(L), tag};
      int so = l < 4 ? WS_H2S + rec * 128 + l * 16 : WS_END;
      PIN(sv); PIN(so);
      rbar();
      ST16(sv, so);
    }
    return;
  }

  const int j = idx - 16, kh = j >> 3, cj = j & 7;
  const bool isO = kh == 0, isF = kh == 1 && cj < 7;
  const int fi = g * 7 + cj;
  const int q = (l & 15) >> 2, p = l & 3;
  if (w >= 8) {
    if (w > 8) return;
    const u32 tag = LD4(WS_EPOCH) + 1u;
    if (isO) {
      if (H3REP == 8) {
#pragma unroll
        for (int i = 0; i < 2; ++i) ST16(E4, WS_H3 + (l >> 3) * 16384 + (i * 8 + g) * 1024 + cj * 128 + (l & 7) * 16);
      } else if (l < 16) ST16(E4, WS_H3 + ((l >> 3) * 8 + g) * 1024 + cj * 128 + (l & 7) * 16);
    } else if (isF) {
#pragma unroll
      for (int i = 0; i < 4; ++i) ST16(E4, WS_H4 + fi * 4096 + i * 1024 + l * 16);
    }
    if (l == 0) *(LAS u32*)(lds + L_FL) = EMPTY;
    rbar();
    VMWAIT(0);
    if (l == 0) ST4(tag, WS_WF + g * 128 + j * 4);
    if (isO) {
      wait_until(t0 + POLL_O);
      const int os = WS_H2S + (g * 16 + (l >> 2)) * 128 + (l & 3) * 16;
      v4u st;
#pragma unroll 1
      for (int it = 0; it < SPIN; ++it) { MEMF(); st = LD16(os); if (none((st.y != tag) | (st.w != tag))) break; __builtin_amdgcn_s_sleep(OSLEEP); }
      const u32 tnow = (u32)__builtin_amdgcn_s_memrealtime();
      bar();
      const float m = asf(st.x);
      float M = fmaxf(m, dpp<ROR4>(m)); M = fmaxf(M, dpp<ROR8>(M)); M = sw16_max(M);
      const float e = __builtin_amdgcn_exp2f((m - M) * LOG2E);
      float L = e * asf(st.z);
      L += dpp<ROR4>(L); L += dpp<ROR8>(L); L = sw16_sum(L);
      *(LAS float*)(lds + L_SC + ((l >> 5) * 4 + (l & 3)) * 32 + ((l >> 2) & 7) * 4) = e * __builtin_amdgcn_rcpf(L);
      MEMF();
      if (l == 0) *(volatile LAS u32*)(lds + L_FL) = 0u;
      if (cj == 0 && l < 8) ST8((v2u{tnow, tag}), WS_OT + l * 1024 + g * 128);
    } else if (isF) {
      wait_until(t0 + POLL_E);
      {
        const int o1 = WS_WF + (l >> 3) * 128 + (l & 7) * 4;
#pragma unroll 1
        for (int it = 0; it < SPIN; ++it) { MEMF(); const u32 t = LD4(o1); if (none(t != tag)) break; __builtin_amdgcn_s_sleep(8); }
      }
      wait_until(t0 + POLL_F);
      const u64 tgo = hint(tag);
      wait_until(tgo + FDELTA);
      rbar();
    }
    return;
  }
  {
    v4u xa, xb, xf;
    u32 ep;
    asm volatile("global_load_dwordx4 %0, %3, %4\n\tglobal_load_dwordx4 %1, %3, %4 offset:512\n\t"
                 "buffer_load_dword %2, %6, %5, 0 offen sc1"
                 : "=&v"(xa), "=&v"(xb), "=&v"(ep)
                 : "v"((l >> 5) * 1024 + (l & 31) * 16), "s"(x), "s"(rs), "v"(w < 4 ? WS_EPOCH : WS_END) : "memory");
    const int kq = j >> 2, cp = j & 3;
    const char* wb = (const char*)wq + (size_t)(kq * 128 + (w & 3) * 32) * 8192 + g * 1024 + cp * 256 + (w >> 2) * 128;
#pragma unroll
    for (int i = 0; i < 4; ++i) dma16(wb, (u32)((8 * i + (l >> 3)) * 8192 + (l & 7) * 16), lbase + L_WQ + w * 4096 + i * 1024);
    asm volatile("s_waitcnt vmcnt(5)" : "+v"(xa), "+v"(xb) :: "memory");
#pragma unroll
    for (int i = 0; i < 4; ++i) xf[i] = __builtin_amdgcn_ds_bpermute(((r & 1) * 32 + (kq & 1) * 16 + (w & 3) * 4 + gm) * 4, kh ? xb[i] : xa[i]);
    float s0 = dot2(xa.x, ONE2, 0.f), s1 = dot2(xb.x, ONE2, 0.f);
    s0 = dot2(xa.y, ONE2, s0); s1 = dot2(xb.y, ONE2, s1); s0 = dot2(xa.z, ONE2, s0); s1 = dot2(xb.z, ONE2, s1);
    s0 = dot2(xa.w, ONE2, s0); s1 = dot2(xb.w, ONE2, s1);
    float sm = s0 + s1;
    sm += dpp<XOR1>(sm); sm += dpp<XOR2>(sm); sm += dpp<ROR4>(sm); sm += dpp<ROR8>(sm); sm = sw16_sum(sm);
    const float mean = sm * (1.f / 512.f);
    float q0 = 0, q1 = 0;
#pragma unroll
    for (int i = 0; i < 4; ++i) {
      const float d0 = lo16(xa[i]) - mean, d1 = hi16(xa[i]) - mean, d2 = lo16(xb[i]) - mean, d3 = hi16(xb[i]) - mean;
      q0 = fmaf(d0, d0, q0); q1 = fmaf(d1, d1, q1); q0 = fmaf(d2, d2, q0); q1 = fmaf(d3, d3, q1);
    }
    float sq = q0 + q1;
    sq += dpp<XOR1>(sq); sq += dpp<XOR2>(sq); sq += dpp<ROR4>(sq); sq += dpp<ROR8>(sq); sq = sw16_sum(sq);
    float mr = rb(mean), rd = __builtin_amdgcn_rcpf(rb(__builtin_amdgcn_sqrtf(rb(rb(sq * (1.f / 512.f)) + 1e-5f))));
    {
      auto a = __builtin_amdgcn_permlane32_swap(asu(mr), asu(mr), false, false);
      auto d = __builtin_amdgcn_permlane32_swap(asu(rd), asu(rd), false, false);
      mr = asf((r & 1) ? a[1] : a[0]); rd = asf((r & 1) ? d[1] : d[0]);
    }
    v4u af;
#pragma unroll
    for (int i = 0; i < 4; ++i) { const u32 d = pk2(lo16(xf[i]) - mr, hi16(xf[i]) - mr); af[i] = pk2(lo16(d) * rd, hi16(d) * rd); }
    v4u am[4];
#pragma unroll
    for (int nt = 0; nt < 4; ++nt) {
#pragma unroll
      for (int i = 0; i < 4; ++i) am[nt][i] = (r >> 2) == nt ? af[i] : 0u;
      PIN(am[nt]);
    }
    const LAS char* tb = lds + L_WQ + w * 4096 + (8 * gm + q) * 128 + 8 * p;
    int so = WS_QG + (g * 2 + (w & 1)) * 8192 + kq * 2048 + (2 * cp + ((w >> 1) & 1)) * 256 + l * 4;
    const LAS char* pr = lds + L_RQ + ((w >> 1) & 1) * 2048 + (w & 1) * 256 + l * 4;
    PIN(tb); PIN(so); PIN(pr);
    asm volatile("s_waitcnt vmcnt(0)" : "+v"(ep) :: "memory");
    v4f D = {0, 0, 0, 0};
#pragma unroll
    for (int nt = 0; nt < 4; ++nt) D = mfma(am[nt], trd2(tb + nt * 32, tb + nt * 32 + 512), D);
    *(LAS float*)(lds + L_RQ + w * 512 + l * 4) = D[0]; *(LAS float*)(lds + L_RQ + w * 512 + 256 + l * 4) = D[1];
    bar();
    if (w < 4) {
      const u32 tb8 = (((ep + 1u) >> ((l & 3) * 4)) ^ ((l & 3) ? ep + 1u : 0u)) & 15u;
      float t[4];
#pragma unroll
      for (int i = 0; i < 4; ++i) t[i] = *(const LAS float*)(pr + i * 512);
      ST4(((asu((t[0] + t[1]) + (t[2] + t[3])) + 8u) & 0xFFFFFFF0u) | tb8, so);
    }
  }
  if (isO) {
    {
      const u32 cls = (((u32)(size_t)wo >> 7) + cj) & 7;
      wait_until(t0 + (cls == 3 ? WO_START - WO3_EARLY : cls == 7 ? WO_START - WO7_EARLY : WO_START));
    }
    {
      const char* wb = (const char*)wo + (size_t)(g * 512 + w * 64) * 1024 + cj * 128;
      const u32 vo = (u32)((((l >> 3) & 1) * 16 + (l >> 4)) * 1024 + (l & 7) * 16);
#pragma unroll
      for (int i = 0; i < 8; ++i) dma16(wb, vo + (u32)(((i >> 2) * 32 + (i & 3) * 4) * 1024), lbase + L_W2 + w * 8192 + i * 1024);
    }
    float xres[2] = {0.f, 0.f};
    if (g == 0) { const u32 xx = *(const GAS u32*)(x + (l >> 5) * 512 + cj * 64 + (l & 31) * 2); xres[0] = lo16(xx); xres[1] = hi16(xx); }
    VMWAIT(0);
    v4u bf[2][4];
    {
      const LAS char* tb = lds + L_W2 + w * 8192 + (8 * gm + q) * 128 + 8 * p;
#pragma unroll
      for (int ks = 0; ks < 2; ++ks)
#pragma unroll
        for (int nt = 0; nt < 4; ++nt) bf[ks][nt] = trd2(tb + ks * 4096 + nt * 32, tb + ks * 4096 + nt * 32 + 512);
    }
    int oo = WS_H2O + (g * 16 + (l >> 5) * 8) * 1024 + w * 128 + (l & 31) * 4, oo2 = oo + 4096;
    int so = WS_H3 + (w & 1) * 65536 + ((l >> 5) * 8 + g) * 1024 + cj * 128 + (l & 31) * 4;
    const LAS char* psc = lds + L_SC + ((l >> 5) * 4 + (w >> 1)) * 32;
    LAS char* phw = lds + L_H + w * 256 + l * 4;
    const LAS char* phr = lds + L_H + w * 256 + (r & 1) * 128 + gm * 16;
    LAS char* prw = lds + L_RED + w * 512 + l * 4;
    const LAS char* prr = lds + L_RED + l * 8;
    u64 m1 = 0xFFFF0000FFFF0000ull, m2 = 0xFFFFFFFF00000000ull;
    int s1 = 16384, s2 = 32768, s3 = 49152;
    PINO(oo); PINO(oo2); PINO(so); PIN(psc); PIN(phw); PIN(phr); PIN(prw); PIN(prr); PIN(xres[0]); PIN(xres[1]);
    PINS(m1); PINS(m2); PINS(s1); PINS(s2); PINS(s3);
    bar();
    v4u a0, a1;
#pragma unroll 1
    for (int it = 0; it < SPIN; ++it) {
      u32 a[8];
      MEMF(); PINO(oo); PINO(oo2);
#pragma unroll
      for (int i = 0; i < 4; ++i) { a[i] = LD4(oo + i * 1024); a[4 + i] = LD4(oo2 + i * 1024); }
      asm volatile("s_waitcnt vmcnt(7)" ::: "memory");
      const u32 fl = *(const volatile LAS u32*)(lds + L_FL);
      const v4f s0 = *(const volatile LAS v4f*)(psc), s4 = *(const volatile LAS v4f*)(psc + 16);
      v2f e0 = v2f{lo16(a[0]), hi16(a[0])} * s0[0];
      e0 += v2f{lo16(a[4]), hi16(a[4])} * s4[0];
#pragma unroll
      for (int i = 1; i < 4; ++i) { SB(); e0 += v2f{lo16(a[i]), hi16(a[i])} * s0[i]; SB(); e0 += v2f{lo16(a[4 + i]), hi16(a[4 + i])} * s4[i]; }
      *(LAS u32*)phw = pk2(e0[0], e0[1]);
      MEMF();
      a0 = *(const LAS v4u*)phr; a1 = *(const LAS v4u*)(phr + 64);
      SB();
      if (none(max3u(max3u(a[0], a[1], a[2]), max3u(a[3], a[4], a[5]), max3u(a[6], a[7], fl)) == EMPTY)) break;
    }
    v4f D[4];
#pragma unroll
    for (int nt = 0; nt < 4; ++nt) D[nt] = mfma(a0, bf[0][nt], v4f{0, 0, 0, 0});
#pragma unroll
    for (int nt = 0; nt < 4; ++nt) D[nt] = mfma(a1, bf[1][nt], D[nt]);
    SB();
    const bool g1 = lane_of(m1), g2 = lane_of(m2);
    const float u0 = g2 ? (g1 ? D[3][0] : D[2][0]) : (g1 ? D[1][0] : D[0][0]);
    const float u1 = g2 ? (g1 ? D[3][1] : D[2][1]) : (g1 ? D[1][1] : D[0][1]);
    *(LAS float*)prw = u0; *(LAS float*)(prw + 256) = u1;
    bar();
    if (w < 2) {
      v2f t[8];
#pragma unroll
      for (int i = 0; i < 8; ++i) t[i] = *(const volatile LAS v2f*)(prr + i * 512);
      const float r0 = (((t[0][0] + t[1][0]) + (t[2][0] + t[3][0])) + ((t[4][0] + t[5][0]) + (t[6][0] + t[7][0]))) + xres[0];
      const float r1 = (((t[0][1] + t[1][1]) + (t[2][1] + t[3][1])) + ((t[4][1] + t[5][1]) + (t[6][1] + t[7][1]))) + xres[1];
      const u32 hv = canon(pk2(r0, r1));
      __builtin_amdgcn_raw_buffer_store_b32(hv, rs, so, 0, 16); __builtin_amdgcn_raw_buffer_store_b32(hv, rs, so, s1, 16);
      __builtin_amdgcn_raw_buffer_store_b32(hv, rs, so, s2, 16); __builtin_amdgcn_raw_buffer_store_b32(hv, rs, so, s3, 16);
    }
    return;
  }
  if (isF) {
    wait_until(t0 + (((((u32)(size_t)w1 >> 6) + fi) & 6) == 6 ? WF_START - WF3_EARLY : WF_START));
    {
      const char* wa = (const char*)w1 + (size_t)(w * 64) * (NF * 2) + fi * 64;
#pragma unroll
      for (int i = 0; i < 4; ++i) dma16(wa, (u32)((16 * i + (l >> 2)) * (NF * 2) + (l & 3) * 16), lbase + L_W2 + w * 8192 + i * 1024);
      const char* wb = (const char*)w2 + (size_t)(fi * 32) * 1024 + w * 128;
#pragma unroll
      for (int i = 0; i < 4; ++i) dma16(wb, (u32)((8 * i + (l >> 3)) * 1024 + (l & 7) * 16), lbase + L_W2 + w * 8192 + 4096 + i * 1024);
    }
    VMWAIT(0);
    v4u f1[2][2], f2[4];
    {
      const v4u ONES = {ONE2, ONE2, ONE2, ONE2};
      const LAS char* ta = lds + L_W2 + w * 8192 + (8 * gm + q) * 64 + 8 * p;
#pragma unroll
      for (int ks = 0; ks < 2; ++ks)
#pragma unroll
        for (int ft = 0; ft < 2; ++ft) f1[ks][ft] = trd2(ta + ks * 2048 + ft * 32, ta + ks * 2048 + ft * 32 + 256);
      const LAS char* tb = lds + L_W2 + w * 8192 + 4096 + (8 * gm + q) * 128 + 8 * p;
#pragma unroll
      for (int nt = 0; nt < 4; ++nt) f2[nt] = trd2(tb + nt * 32, tb + nt * 32 + 512);
      v4f C0 = mfma(ONES, f1[0][0], v4f{0, 0, 0, 0}); C0 = mfma(ONES, f1[1][0], C0);
      v4f C1 = mfma(ONES, f1[0][1], v4f{0, 0, 0, 0}); C1 = mfma(ONES, f1[1][1], C1);
      if (l < 32) *(LAS float*)(lds + L_C + w * 128 + l * 4) = (gm & 1) ? C1[0] : C0[0];
    }
    const LAS char* pcs = lds + L_C + (l & 31) * 4;
    int oh = WS_H3 + rep * 16384 + (l >> 5) * 8192 + w * 128 + (l & 31) * 4, oh2 = oh + 4096;
    int so = WS_H4 + fi * 4096 + w * 512 + l * 8;
    LAS char* phw = lds + L_H + w * 256 + l * 4;
    const LAS char* phr = lds + L_H + w * 256 + (r >> 3) * 128 + gm * 16;
    LAS char* psw = (l & 31) == 0 ? lds + L_ST + (l >> 5) * 64 + w * 8 : lds + L_DUM + l * 8;
    const LAS char* psr = lds + L_ST + (l >> 5) * 64;
    LAS char* prw = lds + L_RED + w * 256 + l * 4;
    const LAS char* prr = lds + L_RED + l * 4;
    LAS char* puw = lds + L_U + w * 128 + l * 2;
    const LAS char* pur = lds + L_U + w * 128 + (r & 1) * 64 + gm * 16;
    u64 m1 = 0xFFFF0000FFFF0000ull, m2 = 0xFFFFFFFF00000000ull;
    PINS(m1); PINS(m2);
    PIN(pcs); PINO(oh); PINO(oh2); PINO(so); PIN(phw); PIN(phr); PIN(psw); PIN(psr); PIN(prw); PIN(prr); PIN(puw); PIN(pur);
    bar();
    float csum;
    u32 hp;
    v4u a0, a1;
#pragma unroll 1
    for (int it = 0; it < SPIN; ++it) {
      u32 a[8];
      MEMF(); PINO(oh); PINO(oh2);
#pragma unroll
      for (int i = 0; i < 4; ++i) { a[i] = LD4(oh + i * 1024); a[4 + i] = LD4(oh2 + i * 1024); SB(); }
      csum = 0;
#pragma unroll
      for (int i = 0; i < 8; ++i) csum += *(const LAS float*)(pcs + i * 128);
      PIN(csum);
      SB();
      v2f t = v2f{lo16(a[0]), hi16(a[0])} + v2f{lo16(a[4]), hi16(a[4])};
#pragma unroll
      for (int i = 1; i < 4; ++i) { SB(); t += v2f{lo16(a[i]), hi16(a[i])}; SB(); t += v2f{lo16(a[4 + i]), hi16(a[4 + i])}; }
      hp = pk2(t[0], t[1]);
      *(LAS u32*)phw = hp;
      MEMF();
      a0 = *(const LAS v4u*)phr; a1 = *(const LAS v4u*)(phr + 64);
      SB();
      if (none(max3u(max3u(a[0], a[1], a[2]), max3u(a[3], a[4], a[5]), max(a[6], a[7])) == EMPTY)) break;
    }
    {
      float ss = dot2(hp, ONE2, 0.f), gs = dot2(hp, hp, 0.f);
      ss += dpp<XOR1>(ss); gs += dpp<XOR1>(gs); ss += dpp<XOR2>(ss); gs += dpp<XOR2>(gs);
      ss += dpp<ROR4>(ss); gs += dpp<ROR4>(gs); ss += dpp<ROR8>(ss); gs += dpp<ROR8>(gs);
      ss = sw16_sum(ss); gs = sw16_sum(gs);
      *(LAS v2f*)psw = v2f{ss, gs};
    }
    SB();
    v4f Z0 = mfma(a0, f1[0][0], v4f{0, 0, 0, 0}), Z1 = mfma(a0, f1[0][1], v4f{0, 0, 0, 0});
    Z0 = mfma(a1, f1[1][0], Z0); Z1 = mfma(a1, f1[1][1], Z1);
    const bool g1 = lane_of(m1), g2 = lane_of(m2);
    *(LAS float*)prw = g1 ? Z1[0] : Z0[0];
    bar();
    float zt[8]; v4f tt[4];
#pragma unroll
    for (int i = 0; i < 4; ++i) tt[i] = *(const LAS v4f*)(psr + i * 16);
#pragma unroll
    for (int i = 0; i < 8; ++i) zt[i] = *(const LAS float*)(prr + i * 256);
    __builtin_amdgcn_sched_barrier(0);
    const float sm = ((tt[0][0] + tt[0][2]) + (tt[1][0] + tt[1][2])) + ((tt[2][0] + tt[2][2]) + (tt[3][0] + tt[3][2]));
    const float sq = ((tt[0][1] + tt[0][3]) + (tt[1][1] + tt[1][3])) + ((tt[2][1] + tt[2][3]) + (tt[3][1] + tt[3][3]));
    const float zr = ((zt[0] + zt[1]) + (zt[2] + zt[3])) + ((zt[4] + zt[5]) + (zt[6] + zt[7]));
    const float mean = sm * (1.f / 512.f);
    const float var = fmaxf(sq * (1.f / 512.f) - mean * mean, 0.f);
    const float mr = rb(mean), rd = __builtin_amdgcn_rcpf(rb(__builtin_amdgcn_sqrtf(rb(rb(var) + 1e-5f))));
    const float z = rb((zr - mr * csum) * rd);
    const float u = z * __builtin_amdgcn_rcpf(1.f + __builtin_amdgcn_exp2f(-z * LOG2E));
    *(LAS u16*)puw = (u16)pk2(u, u);
    MEMF();
    const v4u uf = *(const LAS v4u*)pur;
    const v4f Y0 = mfma(uf, f2[0], v4f{0, 0, 0, 0}), Y1 = mfma(uf, f2[1], v4f{0, 0, 0, 0});
    const v4f Y2 = mfma(uf, f2[2], v4f{0, 0, 0, 0}), Y3 = mfma(uf, f2[3], v4f{0, 0, 0, 0});
    const float y0 = g2 ? (g1 ? Y3[0] : Y2[0]) : (g1 ? Y1[0] : Y0[0]);
    const float y1 = g2 ? (g1 ? Y3[1] : Y2[1]) : (g1 ? Y1[1] : Y0[1]);
    ST8((v2u{canon(asu(y0)), canon(asu(y1))}), so);
    return;
  }
}

extern "C" void kernel_launch(void* const* d_in, const int* in_sizes, int n_in, void* d_out, int out_size, void* d_ws, size_t ws_size,
                              hipStream_t stream) {
  fused_block<<<256, NT, 0, stream>>>((const u16*)d_in[0], (const u16*)d_in[1], (const u16*)d_in[2], (const u16*)d_in[3],
                                      (const u16*)d_in[4], (const u16*)d_in[6], (const u16*)d_in[7], (char*)d_ws, (u16*)d_out);
}
```
